```python
import torch

def _softmax(x, axis=-1):
    return torch.softmax(x, dim=axis)
def _gelu(x):
    return torch.nn.functional.gelu(x, approximate="tanh")
def _silu(x):
    return torch.nn.functional.silu(x)
def _sigmoid(x):
    return torch.sigmoid(x)
def _layernorm(x, axis=-1, eps=1e-5):
    m = x.mean(dim=axis, keepdim=True)
    v = x.var(dim=axis, keepdim=True, unbiased=False)
    return (x - m) / torch.sqrt(v + eps)
def _scatter_add(tgt, idx, src, axis=0):
    return tgt.clone().index_add_(axis, idx, src)
def _segment_sum(src, seg, n):
    out = torch.zeros((n,) + src.shape[1:], dtype=src.dtype)
    return out.index_add_(0, seg, src)
def _pad(x, pw):
    flat = tuple(v for pair in reversed(pw) for v in pair)
    return torch.nn.functional.pad(x, flat)

kernel_name = 'sequential_preLN__mha_block__geglu_ffn__prompt'

def setup_inputs():

    _w = (lambda a: (lambda t: t.to(torch.bfloat16) if t.is_floating_point() else t)(torch.as_tensor(a)))
    def _A_setup():
        B, S, D, H = 1, 2048, 512, 32
        return {
            "x": _w((lambda *s: torch.randn(*s, dtype=torch.bfloat16) * 0.05)(B, S, D)),
            "wqkv": _w((lambda *s: torch.randn(*s, dtype=torch.bfloat16) * 0.05)(D, 3 * D)),
            "wo": _w((lambda *s: torch.randn(*s, dtype=torch.bfloat16) * 0.05)(D, D)),
        }
    _A_in = _A_setup()

    def _B_setup():
        B, S, D, F = 1, 2048, 512, 1792
        return {
            "x": _w((lambda *s: torch.randn(*s, dtype=torch.bfloat16) * 0.05)(B, S, D)),
            "w_gate": _w((lambda *s: torch.randn(*s, dtype=torch.bfloat16) * 0.05)(D, F)),
            "w_up": _w((lambda *s: torch.randn(*s, dtype=torch.bfloat16) * 0.05)(D, F)),
            "w_down": _w((lambda *s: torch.randn(*s, dtype=torch.bfloat16) * 0.05)(F, D)),
        }
    _B_in = _B_setup()

    return {
        "A_x": _A_in["x"],
        "A_wqkv": _A_in["wqkv"],
        "A_wo": _A_in["wo"],
        "B_x": _B_in["x"],
        "B_w_gate": _B_in["w_gate"],
        "B_w_up": _B_in["w_up"],
        "B_w_down": _B_in["w_down"],
    }

def reference(A_x, A_wqkv, A_wo, B_x, B_w_gate, B_w_up, B_w_down):
    def _A_ref(x, wqkv, wo):
        B, S, D = x.shape
        H = 32
        Dh = D // H
        qkv = (lambda x, *d: x.permute(*d))((x @ wqkv).reshape(B, S, 3, H, Dh), 2, 0, 3, 1, 4)
        q, k, v = qkv[0], qkv[1], qkv[2]
        scores = torch.einsum("bhqd,bhkd->bhqk", q, k) * (Dh ** -0.5)
        p = _softmax(scores, axis=-1)
        o = torch.einsum("bhqk,bhkd->bhqd", p, v)
        return (lambda x, *d: x.permute(*d))(o, 0, 2, 1, 3).reshape(B, S, D) @ wo

    def _B_ref(x, w_gate, w_up, w_down):
        return (_gelu(x @ w_gate) * (x @ w_up)) @ w_down

    x = A_x
    h = x + _A_ref(_layernorm(x, axis=-1), A_wqkv, A_wo)
    h2 = h + _B_ref(_layernorm(h, axis=-1), B_w_gate, B_w_up, B_w_down)
    return h2
```

```cpp
#include <hip/hip_runtime.h>
#include <stdint.h>

typedef __bf16 bf16x8 __attribute__((ext_vector_type(8)));
typedef __bf16 bf16x16 __attribute__((ext_vector_type(16)));
typedef __bf16 bf16x2 __attribute__((ext_vector_type(2)));
typedef short s16x4 __attribute__((ext_vector_type(4)));
typedef short s16x8 __attribute__((ext_vector_type(8)));
typedef float f32x2 __attribute__((ext_vector_type(2)));
typedef float f32x4 __attribute__((ext_vector_type(4)));
typedef float f32x8 __attribute__((ext_vector_type(8)));
typedef float f32x16 __attribute__((ext_vector_type(16)));
typedef unsigned u32x2 __attribute__((ext_vector_type(2)));
typedef unsigned u32x4 __attribute__((ext_vector_type(4)));
typedef unsigned long long u64;
#define LAS __attribute__((address_space(3)))
#define GAS __attribute__((address_space(1)))
typedef LAS char* lptr;

constexpr int DM = 512, FF = 1792;
constexpr unsigned WS_Q = 0, WS_K = 2u << 20, WS_V = 4u << 20, WS_O = 6u << 20, WS_H = 8u << 20, WS_A = 10u << 20;
constexpr unsigned WS_WO = 17u << 20, WS_WD = WS_WO + (512u << 10), WS_WG = WS_WD + 1792u * 1024u;
constexpr unsigned WS_KMAX = 23u << 20, WS_HST = WS_KMAX + (64u << 10)  , WS_SYNC = 24u << 20, WS_END = 26u << 20;
constexpr unsigned SY_EPOCH = 0, SY_FW = 4096, SY_F1 = SY_FW + 256 * 128  , SY_F2 = SY_F1 + 256 * 128  ,
                   SY_F3 = SY_F2 + 2048 * 128  , SY_F4 = SY_F3 + 512 * 128  ;
constexpr float QSCALE = 0.25f * 1.4426950408889634f;
constexpr float LN_EPS = 1e-5f;
constexpr float SAFE_BOUND = 40.f;
constexpr unsigned SPIN_MEM = 1u << 17, SPIN_LDS = 1u << 21;
constexpr int CODE_LINES = 412;
constexpr int LDS_BYTES = 163840, LDS_CTL = 163840 - 1024;

#define MFMA32(a, b, c) __builtin_amdgcn_mfma_f32_32x32x16_bf16((a), (b), (c), 0, 0, 0)
#define MFMA16(a, b, c) __builtin_amdgcn_mfma_f32_16x16x32_bf16((a), (b), (c), 0, 0, 0)
#define SMF(a, b, c) __builtin_amdgcn_smfmac_f32_32x32x32_bf16((a), __builtin_bit_cast(bf16x16, (b)), (c), (int)0xDDDDDDDD, 0, 0)
#define BARRIER() asm volatile("s_waitcnt lgkmcnt(0)\n\ts_barrier" ::: "memory")
#define WAITV(n) asm volatile("s_waitcnt vmcnt(%0)" :: "i"(n) : "memory")
#define SBAR() __builtin_amdgcn_sched_barrier(0)
#define PIN(x) asm volatile("" : "+v"(x))
#define RLX __ATOMIC_RELAXED, __HIP_MEMORY_SCOPE_AGENT

#define P1T 0
#define TCH 1
#define YEARLY 0
#define ROT 4
#define DLY 16
#define PSL 1
template <int I> struct IC { static constexpr int v = I; };
template <int I, int N, class F> __device__ __forceinline__ void sfor(F&& f) { if constexpr (I < N) { f(IC<I>{}); sfor<I + 1, N>(f); } }
__device__ __forceinline__ void dma16(unsigned voff, const void* sbase, unsigned ldsaddr) {
  asm volatile("s_mov_b32 m0, %2\n\ts_nop 0\n\tglobal_load_lds_dwordx4 %0, %1" :: "v"(voff), "s"(sbase), "s"(ldsaddr) : "memory");
}
__device__ __forceinline__ void dma16c(unsigned voff, const void* sbase, unsigned ldsaddr) {
  asm volatile("s_mov_b32 m0, %2\n\ts_nop 0\n\tglobal_load_lds_dwordx4 %0, %1 sc1" :: "v"(voff), "s"(sbase), "s"(ldsaddr) : "memory");
}
__device__ __forceinline__ void dma4c(unsigned voff, const void* sbase, unsigned ldsaddr) {
  asm volatile("s_mov_b32 m0, %2\n\ts_nop 0\n\tglobal_load_lds_dwordx4 %0, %1 sc1\n\tglobal_load_lds_dwordx4 %0, %1 offset:1024 sc1\n\t"
               "global_load_lds_dwordx4 %0, %1 offset:2048 sc1\n\tglobal_load_lds_dwordx4 %0, %1 offset:3072 sc1" :: "v"(voff), "s"(sbase), "s"(ldsaddr) : "memory");
}
__device__ __forceinline__ void dma3c(unsigned voff, const void* sbase, unsigned ldsaddr) {
  asm volatile("s_mov_b32 m0, %2\n\ts_nop 0\n\tglobal_load_lds_dwordx4 %0, %1 sc1\n\tglobal_load_lds_dwordx4 %0, %1 offset:1024 sc1\n\t"
               "global_load_lds_dwordx4 %0, %1 offset:2048 sc1" :: "v"(voff), "s"(sbase), "s"(ldsaddr) : "memory");
}
__device__ __forceinline__ void dma2c(unsigned voff, const void* sbase, unsigned ldsaddr) {
  asm volatile("s_mov_b32 m0, %2\n\ts_nop 0\n\tglobal_load_lds_dwordx4 %0, %1 sc1\n\tglobal_load_lds_dwordx4 %0, %1 offset:1024 sc1" :: "v"(voff), "s"(sbase), "s"(ldsaddr) : "memory");
}
__device__ __forceinline__ void dma3v(unsigned v0, unsigned v1, unsigned v2, const void* sbase, unsigned ldsaddr) {
  asm volatile("s_mov_b32 m0, %4\n\ts_nop 0\n\tglobal_load_lds_dwordx4 %0, %3\n\tglobal_load_lds_dwordx4 %1, %3 offset:1024\n\tglobal_load_lds_dwordx4 %2, %3 offset:2048"
               :: "v"(v0), "v"(v1), "v"(v2), "s"(sbase), "s"(ldsaddr) : "memory");
}
__device__ __forceinline__ void TOUCH(const void* base, unsigned line) {
  asm volatile("s_mov_b32 m0, %2\n\ts_nop 0\n\tglobal_load_lds_dword %0, %1 sc1" :: "v"(line * 128), "s"(base), "s"((unsigned)(LDS_CTL + 512)) : "memory");
}
typedef __amdgpu_buffer_rsrc_t rsrc_t;
__device__ __forceinline__ u32x4 ldc16(rsrc_t r, unsigned voff, unsigned soff) { return __builtin_amdgcn_raw_buffer_load_b128(r, voff, soff, 16); }
__device__ __forceinline__ u32x2 ldc8(rsrc_t r, unsigned voff, unsigned soff) { return __builtin_amdgcn_raw_buffer_load_b64(r, voff, soff, 16); }
__device__ __forceinline__ unsigned ldc4(rsrc_t r, unsigned voff, unsigned soff) { return __builtin_amdgcn_raw_buffer_load_b32(r, voff, soff, 16); }
__device__ __forceinline__ void stc16(rsrc_t r, unsigned voff, unsigned soff, u32x4 v) { __builtin_amdgcn_raw_buffer_store_b128(v, r, voff + soff, 0, 16); }
__device__ __forceinline__ void stc8(rsrc_t r, unsigned voff, unsigned soff, u32x2 v) { __builtin_amdgcn_raw_buffer_store_b64(v, r, voff, soff, 16); }
__device__ __forceinline__ void stc4(rsrc_t r, unsigned voff, unsigned soff, unsigned v) { __builtin_amdgcn_raw_buffer_store_b32(v, r, voff, soff, 16); }
__device__ __forceinline__ unsigned pk2(float lo, float hi) { f32x2 v = {lo, hi}; return __builtin_bit_cast(unsigned, __builtin_convertvector(v, bf16x2)); }
__device__ __forceinline__ float bflo(unsigned u) { return __uint_as_float(u << 16); }
__device__ __forceinline__ float bfhi(unsigned u) { return __uint_as_float(u & 0xffff0000u); }
__device__ __forceinline__ float swap32_add(float v) { auto r = __builtin_amdgcn_permlane32_swap(__float_as_uint(v), __float_as_uint(v), false, false); return __uint_as_float(r[0]) + __uint_as_float(r[1]); }
__device__ __forceinline__ float swap32_max(float v) { auto r = __builtin_amdgcn_permlane32_swap(__float_as_uint(v), __float_as_uint(v), false, false); return fmaxf(__uint_as_float(r[0]), __uint_as_float(r[1])); }
__device__ __forceinline__ bf16x8 lds_frag(lptr p) { return *(LAS const bf16x8*)p; }
__device__ __forceinline__ bf16x8 as_frag(u32x4 v) { return __builtin_bit_cast(bf16x8, v); }
__device__ __forceinline__ s16x4 lds_tr(lptr p) { return __builtin_amdgcn_ds_read_tr16_b64_v4i16((LAS s16x4*)p); }
__device__ __forceinline__ bf16x8 join(s16x4 a, s16x4 b) { s16x8 v = {a[0], a[1], a[2], a[3], b[0], b[1], b[2], b[3]}; return __builtin_bit_cast(bf16x8, v); }
__device__ __forceinline__ f32x8 lo8(const f32x16& v) { return __builtin_shufflevector(v, v, 0, 1, 2, 3, 4, 5, 6, 7); }
__device__ __forceinline__ f32x8 hi8(const f32x16& v) { return __builtin_shufflevector(v, v, 8, 9, 10, 11, 12, 13, 14, 15); }
__device__ __forceinline__ float wave_max_nonneg(float v) {
  int x = __float_as_int(v) & 0x7fffffff;
  x = max(x, __builtin_amdgcn_update_dpp(0, x, 0x128, 0xf, 0xf, false));
  x = max(x, __builtin_amdgcn_update_dpp(0, x, 0x124, 0xf, 0xf, false));
  x = max(x, __builtin_amdgcn_update_dpp(0, x, 0x122, 0xf, 0xf, false));
  x = max(x, __builtin_amdgcn_update_dpp(0, x, 0x121, 0xf, 0xf, false));
  const int a = max(__builtin_amdgcn_readlane(x, 0), __builtin_amdgcn_readlane(x, 16)), b = max(__builtin_amdgcn_readlane(x, 32), __builtin_amdgcn_readlane(x, 48));
  return __int_as_float(max(a, b));
}
__device__ __forceinline__ unsigned lds_arrive(lptr ctr, int lane) {
  unsigned old = 0;
  asm volatile("s_waitcnt lgkmcnt(0)" ::: "memory");
  if (lane == 0) old = __hip_atomic_fetch_add((LAS unsigned*)ctr, 1u, __ATOMIC_RELAXED, __HIP_MEMORY_SCOPE_WORKGROUP);
  old = __builtin_amdgcn_readfirstlane(old);
  asm volatile("" ::: "memory");
  return old;
}
__device__ __forceinline__ void lds_wait(lptr ctr, unsigned target) {
#pragma unroll 1
  for (unsigned spin = 0; spin < SPIN_LDS; ++spin)
    if (__builtin_amdgcn_readfirstlane(__hip_atomic_load((LAS unsigned*)ctr, __ATOMIC_RELAXED, __HIP_MEMORY_SCOPE_WORKGROUP)) >= target) break;
  asm volatile("" ::: "memory");
}
__device__ __forceinline__ void lds_wait_slow(lptr ctr, unsigned target) {
#pragma unroll 1
  for (unsigned spin = 0; spin < SPIN_LDS; ++spin) {
    if (__builtin_amdgcn_readfirstlane(__hip_atomic_load((LAS unsigned*)ctr, __ATOMIC_RELAXED, __HIP_MEMORY_SCOPE_WORKGROUP)) >= target) break;
    __builtin_amdgcn_s_sleep(2); }
  asm volatile("" ::: "memory");
}
__device__ __forceinline__ void lds_signal(lptr ctr, int lane) {
  asm volatile("s_waitcnt lgkmcnt(0)" ::: "memory");
  if (lane == 0) __hip_atomic_fetch_add((LAS unsigned*)ctr, 1u, __ATOMIC_RELAXED, __HIP_MEMORY_SCOPE_WORKGROUP);
  asm volatile("" ::: "memory");
}
__device__ __forceinline__ unsigned word_addr(int word_off, int lane) { unsigned a = lane ? (unsigned)(LDS_CTL + 768 + lane * 4) : (unsigned)(LDS_CTL + word_off); asm volatile("" : "+v"(a)); return a; }
__device__ __forceinline__ void word_set(lptr lds, unsigned a, unsigned v) { __hip_atomic_store((LAS unsigned*)(lds + a), v, __ATOMIC_RELAXED, __HIP_MEMORY_SCOPE_WORKGROUP); }
__device__ __forceinline__ void flag_set(char* flag, u64 tag, int lane) { if (lane == 0) __hip_atomic_store((GAS u64*)flag, tag, RLX); }
__device__ __forceinline__ void poll(char* flags, int n, u64 tag, int lane) {
  GAS u64* p = (GAS u64*)(flags + (lane < n ? lane : 0) * 128);
#pragma unroll 1
  for (unsigned spin = 0; spin < SPIN_MEM; ++spin) {
    if (__all(__hip_atomic_load(p, RLX) == tag)) break;
    if (PSL) __builtin_amdgcn_s_sleep(PSL);
  }
  asm volatile("" ::: "memory");
}

__device__ __forceinline__ void poll1(char* f0, u64 tag) {
#pragma unroll 1
  for (unsigned spin = 0; spin < SPIN_MEM; ++spin) {
    if (__all(__hip_atomic_load((GAS u64*)f0, RLX) == tag)) break;
    if (PSL) __builtin_amdgcn_s_sleep(PSL);
  }
  asm volatile("" ::: "memory");
}
__device__ __forceinline__ void poll2x(char* f0, char* f1, u64 tag) {
#pragma unroll 1
  for (unsigned spin = 0; spin < SPIN_MEM; ++spin) {
    const u64 a = __hip_atomic_load((GAS u64*)f0, RLX), b = __hip_atomic_load((GAS u64*)f1, RLX);
    if (__all(a == tag && b == tag)) break;
    if (PSL) __builtin_amdgcn_s_sleep(PSL);
  }
  asm volatile("" ::: "memory");
}
__device__ __forceinline__ void poll3x(char* f0, char* f1, char* f2, u64 tag) {
#pragma unroll 1
  for (unsigned spin = 0; spin < SPIN_MEM; ++spin) {
    const u64 a = __hip_atomic_load((GAS u64*)f0, RLX), b = __hip_atomic_load((GAS u64*)f1, RLX), c = __hip_atomic_load((GAS u64*)f2, RLX);
    if (__all(a == tag && b == tag && c == tag)) break;
    if (PSL) __builtin_amdgcn_s_sleep(PSL);
  }
  asm volatile("" ::: "memory");
}

#define STAMP(i)
#define STAMP_DECL
#define STAMP_FLUSH()

constexpr int P1_SLOT = 32768, P1_SCR = 136 * 1024;
constexpr int AT_CH = 32768, AT_ONE = 128 * 1024, AT_X = 136 * 1024;
constexpr int P3_O = 95 * 1024;
__host__ __device__ constexpr int gu_slot(int stage, int s, int i) { return stage == 0 ? 3 * s + i : stage == 1 ? (s < 29 ? 99 + 2 * s + i : 2 * (s - 29) + i) : 8 + 2 * s + i; }
constexpr int P4_ST = 157 * 1024;
__host__ __device__ constexpr int p5_pos(int c, int i) { return (c >> 3) * 56 + (i < 3 ? 3 * (c & 7) + i : i < 5 ? 21 + 2 * (c & 7) + i : 35 + 2 * (c & 7) + i); }
constexpr int P5_W = 95 * 1024;
__host__ __device__ constexpr int p5_slot(int r) { return r == 10 ? P5_W : r == 12 ? P5_W + 16384 : r == 11 ? 78 * 1024 : r == 13 ? 0 : r % 5 == 0 ? 78 * 1024 : (r % 5 - 1) * 16384; }
enum { C_ST = 0, C_F1 = 4, C_RT = 8, C_P4 = 12, C_RDY = 16  , C_DONE = 32  , C_GO = 48  , C_X = 64, C_F3 = 68  , C_H = 80  , C_S3 = 96  , C_L1 = 104, C_L2 = 108, C_FW = 112, C_D3 = 116, C_GB = 120  , C_RS = 76 };

#define HWIN 8

#define NWD 24
#define TOUCHA 1
#define PACE 4
#define RELANE() unsigned lq_ = l16; asm volatile("" : "+v"(lq_)); const int lane = lq_ >> 4, t = lane & 31, h = lane >> 5; (void)t; (void)h
#define AT_TXT0 \
    "v_exp_f32 v128, v128\n\tv_exp_f32 v129, v129\n\tv_smfmac_f32_32x32x32_bf16 v[176:191], v[232:235], v[152:159], v240\n\tv_exp_f32 v130, v130\n\t" \
    "v_exp_f32 v131, v131\n\tv_exp_f32 v132, v132\n\tv_exp_f32 v133, v133\n\tv_mfma_f32_32x32x16_bf16 v[144:159], v[228:231], v[212:215], v[192:207]\n\t" \
    "v_exp_f32 v134, v134\n\tv_exp_f32 v135, v135\n\tv_exp_f32 v136, v136\n\tv_exp_f32 v137, v137\n\t" \
    "v_exp_f32 v138, v138\n\ts_waitcnt lgkmcnt(0)\n\tv_smfmac_f32_32x32x32_bf16 v[160:175], v[224:227], v[128:135], v240\n\tds_read_b128 v[232:235], v242 offset:1536\n\t" \
    "ds_read_b128 v[236:239], v242 offset:1024\n\tv_exp_f32 v139, v139\n\tv_exp_f32 v140, v140\n\tv_exp_f32 v141, v141\n\t" \
    "v_exp_f32 v142, v142\n\tv_exp_f32 v143, v143\n\tv_exp_f32 v144, v144\n\tv_exp_f32 v145, v145\n\t" \
    "v_smfmac_f32_32x32x32_bf16 v[160:175], v[220:223], v[136:143], v240\n\tv_exp_f32 v146, v146\n\tv_exp_f32 v147, v147\n\tv_exp_f32 v148, v148\n\t" \
    "v_exp_f32 v149, v149\n\tv_mfma_f32_32x32x16_bf16 v[128:143], v[216:219], v[208:211], v[192:207]\n\tv_exp_f32 v150, v150\n\tv_exp_f32 v151, v151\n\t" \
    "v_exp_f32 v152, v152\n\tv_exp_f32 v153, v153\n\tv_exp_f32 v154, v154\n\tv_smfmac_f32_32x32x32_bf16 v[176:191], v[224:227], v[144:151], v240\n\t" \
    "ds_read_b128 v[228:231], v241 offset:2048\n\tv_exp_f32 v155, v155\n\tv_exp_f32 v156, v156\n\tv_exp_f32 v157, v157\n\t" \
    "v_exp_f32 v158, v158\n\tv_exp_f32 v159, v159\n\t" \
    ""
#define AT_TXT1 \
    "v_exp_f32 v128, v128\n\tv_exp_f32 v129, v129\n\tv_smfmac_f32_32x32x32_bf16 v[176:191], v[220:223], v[152:159], v240\n\tv_exp_f32 v130, v130\n\t" \
    "v_exp_f32 v131, v131\n\tv_exp_f32 v132, v132\n\tv_exp_f32 v133, v133\n\tv_mfma_f32_32x32x16_bf16 v[144:159], v[216:219], v[212:215], v[192:207]\n\t" \
    "v_exp_f32 v134, v134\n\tv_exp_f32 v135, v135\n\tv_exp_f32 v136, v136\n\tv_exp_f32 v137, v137\n\t" \
    "v_exp_f32 v138, v138\n\ts_waitcnt lgkmcnt(0)\n\tv_smfmac_f32_32x32x32_bf16 v[160:175], v[236:239], v[128:135], v240\n\tds_read_b128 v[220:223], v242 offset:2560\n\t" \
    "ds_read_b128 v[224:227], v242 offset:2048\n\tv_exp_f32 v139, v139\n\tv_exp_f32 v140, v140\n\tv_exp_f32 v141, v141\n\t" \
    "v_exp_f32 v142, v142\n\tv_exp_f32 v143, v143\n\tv_exp_f32 v144, v144\n\tv_exp_f32 v145, v145\n\t" \
    "v_smfmac_f32_32x32x32_bf16 v[160:175], v[232:235], v[136:143], v240\n\tv_exp_f32 v146, v146\n\tv_exp_f32 v147, v147\n\tv_exp_f32 v148, v148\n\t" \
    "v_exp_f32 v149, v149\n\tv_mfma_f32_32x32x16_bf16 v[128:143], v[228:231], v[208:211], v[192:207]\n\tv_exp_f32 v150, v150\n\tv_exp_f32 v151, v151\n\t" \
    "v_exp_f32 v152, v152\n\tv_exp_f32 v153, v153\n\tv_exp_f32 v154, v154\n\tv_smfmac_f32_32x32x32_bf16 v[176:191], v[236:239], v[144:151], v240\n\t" \
    "ds_read_b128 v[216:219], v241 offset:3072\n\tv_exp_f32 v155, v155\n\tv_exp_f32 v156, v156\n\tv_exp_f32 v157, v157\n\t" \
    "v_exp_f32 v158, v158\n\tv_exp_f32 v159, v159\n\t" \
    ""
#define AT_TXT2 \
    "v_exp_f32 v128, v128\n\tv_exp_f32 v129, v129\n\tv_smfmac_f32_32x32x32_bf16 v[176:191], v[232:235], v[152:159], v240\n\tv_exp_f32 v130, v130\n\t" \
    "v_exp_f32 v131, v131\n\tv_exp_f32 v132, v132\n\tv_exp_f32 v133, v133\n\tv_mfma_f32_32x32x16_bf16 v[144:159], v[228:231], v[212:215], v[192:207]\n\t" \
    "v_exp_f32 v134, v134\n\tv_exp_f32 v135, v135\n\tv_exp_f32 v136, v136\n\tv_exp_f32 v137, v137\n\t" \
    "v_exp_f32 v138, v138\n\ts_waitcnt lgkmcnt(0)\n\tv_smfmac_f32_32x32x32_bf16 v[160:175], v[224:227], v[128:135], v240\n\tds_read_b128 v[232:235], v242 offset:3584\n\t" \
    "ds_read_b128 v[236:239], v242 offset:3072\n\tv_exp_f32 v139, v139\n\tv_exp_f32 v140, v140\n\tv_exp_f32 v141, v141\n\t" \
    "v_exp_f32 v142, v142\n\tv_exp_f32 v143, v143\n\tv_exp_f32 v144, v144\n\tv_exp_f32 v145, v145\n\t" \
    "v_smfmac_f32_32x32x32_bf16 v[160:175], v[220:223], v[136:143], v240\n\tv_exp_f32 v146, v146\n\tv_exp_f32 v147, v147\n\tv_exp_f32 v148, v148\n\t" \
    "v_exp_f32 v149, v149\n\tv_mfma_f32_32x32x16_bf16 v[128:143], v[216:219], v[208:211], v[192:207]\n\tv_exp_f32 v150, v150\n\tv_exp_f32 v151, v151\n\t" \
    "v_exp_f32 v152, v152\n\tv_exp_f32 v153, v153\n\tv_exp_f32 v154, v154\n\tv_smfmac_f32_32x32x32_bf16 v[176:191], v[224:227], v[144:151], v240\n\t" \
    "ds_read_b128 v[228:231], v241 offset:4096\n\tv_exp_f32 v155, v155\n\tv_exp_f32 v156, v156\n\tv_exp_f32 v157, v157\n\t" \
    "v_exp_f32 v158, v158\n\tv_exp_f32 v159, v159\n\t" \
    ""
#define AT_TXT3 \
    "v_exp_f32 v128, v128\n\tv_exp_f32 v129, v129\n\tv_smfmac_f32_32x32x32_bf16 v[176:191], v[220:223], v[152:159], v240\n\tv_exp_f32 v130, v130\n\t" \
    "v_exp_f32 v131, v131\n\tv_exp_f32 v132, v132\n\tv_exp_f32 v133, v133\n\tv_mfma_f32_32x32x16_bf16 v[144:159], v[216:219], v[212:215], v[192:207]\n\t" \
    "v_exp_f32 v134, v134\n\tv_exp_f32 v135, v135\n\tv_exp_f32 v136, v136\n\tv_exp_f32 v137, v137\n\t" \
    "v_exp_f32 v138, v138\n\ts_waitcnt lgkmcnt(0)\n\tv_smfmac_f32_32x32x32_bf16 v[160:175], v[236:239], v[128:135], v240\n\tds_read_b128 v[220:223], v242 offset:4608\n\t" \
    "ds_read_b128 v[224:227], v242 offset:4096\n\tv_exp_f32 v139, v139\n\tv_exp_f32 v140, v140\n\tv_exp_f32 v141, v141\n\t" \
    "v_exp_f32 v142, v142\n\tv_exp_f32 v143, v143\n\tv_exp_f32 v144, v144\n\tv_exp_f32 v145, v145\n\t" \
    "v_smfmac_f32_32x32x32_bf16 v[160:175], v[232:235], v[136:143], v240\n\tv_exp_f32 v146, v146\n\tv_exp_f32 v147, v147\n\tv_exp_f32 v148, v148\n\t" \
    "v_exp_f32 v149, v149\n\tv_mfma_f32_32x32x16_bf16 v[128:143], v[228:231], v[208:211], v[192:207]\n\tv_exp_f32 v150, v150\n\tv_exp_f32 v151, v151\n\t" \
    "v_exp_f32 v152, v152\n\tv_exp_f32 v153, v153\n\tv_exp_f32 v154, v154\n\tv_smfmac_f32_32x32x32_bf16 v[176:191], v[236:239], v[144:151], v240\n\t" \
    "ds_read_b128 v[216:219], v241 offset:5120\n\tv_exp_f32 v155, v155\n\tv_exp_f32 v156, v156\n\tv_exp_f32 v157, v157\n\t" \
    "v_exp_f32 v158, v158\n\tv_exp_f32 v159, v159\n\t" \
    ""
#define AT_TXT4 \
    "v_exp_f32 v128, v128\n\tv_exp_f32 v129, v129\n\tv_smfmac_f32_32x32x32_bf16 v[176:191], v[232:235], v[152:159], v240\n\tv_exp_f32 v130, v130\n\t" \
    "v_exp_f32 v131, v131\n\tv_exp_f32 v132, v132\n\tv_exp_f32 v133, v133\n\tv_mfma_f32_32x32x16_bf16 v[144:159], v[228:231], v[212:215], v[192:207]\n\t" \
    "v_exp_f32 v134, v134\n\tv_exp_f32 v135, v135\n\tv_exp_f32 v136, v136\n\tv_exp_f32 v137, v137\n\t" \
    "v_exp_f32 v138, v138\n\ts_waitcnt lgkmcnt(0)\n\tv_smfmac_f32_32x32x32_bf16 v[160:175], v[224:227], v[128:135], v240\n\tds_read_b128 v[232:235], v242 offset:5632\n\t" \
    "ds_read_b128 v[236:239], v242 offset:5120\n\tv_exp_f32 v139, v139\n\tv_exp_f32 v140, v140\n\tv_exp_f32 v141, v141\n\t" \
    "v_exp_f32 v142, v142\n\tv_exp_f32 v143, v143\n\tv_exp_f32 v144, v144\n\tv_exp_f32 v145, v145\n\t" \
    "v_smfmac_f32_32x32x32_bf16 v[160:175], v[220:223], v[136:143], v240\n\tv_exp_f32 v146, v146\n\tv_exp_f32 v147, v147\n\tv_exp_f32 v148, v148\n\t" \
    "v_exp_f32 v149, v149\n\tv_mfma_f32_32x32x16_bf16 v[128:143], v[216:219], v[208:211], v[192:207]\n\tv_exp_f32 v150, v150\n\tv_exp_f32 v151, v151\n\t" \
    "v_exp_f32 v152, v152\n\tv_exp_f32 v153, v153\n\tv_exp_f32 v154, v154\n\tv_smfmac_f32_32x32x32_bf16 v[176:191], v[224:227], v[144:151], v240\n\t" \
    "ds_read_b128 v[228:231], v241 offset:6144\n\tv_exp_f32 v155, v155\n\tv_exp_f32 v156, v156\n\tv_exp_f32 v157, v157\n\t" \
    "v_exp_f32 v158, v158\n\tv_exp_f32 v159, v159\n\t" \
    ""
#define AT_TXT5 \
    "v_exp_f32 v128, v128\n\tv_exp_f32 v129, v129\n\tv_smfmac_f32_32x32x32_bf16 v[176:191], v[220:223], v[152:159], v240\n\tv_exp_f32 v130, v130\n\t" \
    "v_exp_f32 v131, v131\n\tv_exp_f32 v132, v132\n\tv_exp_f32 v133, v133\n\tv_mfma_f32_32x32x16_bf16 v[144:159], v[216:219], v[212:215], v[192:207]\n\t" \
    "v_exp_f32 v134, v134\n\tv_exp_f32 v135, v135\n\tv_exp_f32 v136, v136\n\tv_exp_f32 v137, v137\n\t" \
    "v_exp_f32 v138, v138\n\ts_waitcnt lgkmcnt(0)\n\tv_smfmac_f32_32x32x32_bf16 v[160:175], v[236:239], v[128:135], v240\n\tds_read_b128 v[220:223], v242 offset:6656\n\t" \
    "ds_read_b128 v[224:227], v242 offset:6144\n\tv_exp_f32 v139, v139\n\tv_exp_f32 v140, v140\n\tv_exp_f32 v141, v141\n\t" \
    "v_exp_f32 v142, v142\n\tv_exp_f32 v143, v143\n\tv_exp_f32 v144, v144\n\tv_exp_f32 v145, v145\n\t" \
    "v_smfmac_f32_32x32x32_bf16 v[160:175], v[232:235], v[136:143], v240\n\tv_exp_f32 v146, v146\n\tv_exp_f32 v147, v147\n\tv_exp_f32 v148, v148\n\t" \
    "v_exp_f32 v149, v149\n\tv_mfma_f32_32x32x16_bf16 v[128:143], v[228:231], v[208:211], v[192:207]\n\tv_exp_f32 v150, v150\n\tv_exp_f32 v151, v151\n\t" \
    "v_exp_f32 v152, v152\n\tv_exp_f32 v153, v153\n\tv_exp_f32 v154, v154\n\tv_smfmac_f32_32x32x32_bf16 v[176:191], v[236:239], v[144:151], v240\n\t" \
    "ds_read_b128 v[216:219], v241 offset:7168\n\tv_exp_f32 v155, v155\n\tv_exp_f32 v156, v156\n\tv_exp_f32 v157, v157\n\t" \
    "v_exp_f32 v158, v158\n\tv_exp_f32 v159, v159\n\t" \
    ""
#define AT_TXT6 \
    "v_exp_f32 v128, v128\n\tv_exp_f32 v129, v129\n\tv_smfmac_f32_32x32x32_bf16 v[176:191], v[232:235], v[152:159], v240\n\tv_exp_f32 v130, v130\n\t" \
    "v_exp_f32 v131, v131\n\tv_exp_f32 v132, v132\n\tv_exp_f32 v133, v133\n\tv_mfma_f32_32x32x16_bf16 v[144:159], v[228:231], v[212:215], v[192:207]\n\t" \
    "v_exp_f32 v134, v134\n\tv_exp_f32 v135, v135\n\tv_exp_f32 v136, v136\n\tv_exp_f32 v137, v137\n\t" \
    "v_exp_f32 v138, v138\n\ts_waitcnt lgkmcnt(0)\n\tv_smfmac_f32_32x32x32_bf16 v[160:175], v[224:227], v[128:135], v240\n\tds_read_b128 v[232:235], v242 offset:7680\n\t" \
    "ds_read_b128 v[236:239], v242 offset:7168\n\tv_exp_f32 v139, v139\n\tv_exp_f32 v140, v140\n\tv_exp_f32 v141, v141\n\t" \
    "v_exp_f32 v142, v142\n\tv_exp_f32 v143, v143\n\tv_exp_f32 v144, v144\n\tv_exp_f32 v145, v145\n\t" \
    "v_smfmac_f32_32x32x32_bf16 v[160:175], v[220:223], v[136:143], v240\n\tv_exp_f32 v146, v146\n\tv_exp_f32 v147, v147\n\tv_exp_f32 v148, v148\n\t" \
    "v_exp_f32 v149, v149\n\tv_mfma_f32_32x32x16_bf16 v[128:143], v[216:219], v[208:211], v[192:207]\n\tv_exp_f32 v150, v150\n\tv_exp_f32 v151, v151\n\t" \
    "v_exp_f32 v152, v152\n\tv_exp_f32 v153, v153\n\tv_exp_f32 v154, v154\n\tv_smfmac_f32_32x32x32_bf16 v[176:191], v[224:227], v[144:151], v240\n\t" \
    "ds_read_b128 v[228:231], v241 offset:32768\n\tv_exp_f32 v155, v155\n\tv_exp_f32 v156, v156\n\tv_exp_f32 v157, v157\n\t" \
    "v_exp_f32 v158, v158\n\tv_exp_f32 v159, v159\n\t" \
    ""
#define AT_TXT7 \
    "v_exp_f32 v128, v128\n\tv_exp_f32 v129, v129\n\tv_smfmac_f32_32x32x32_bf16 v[176:191], v[220:223], v[152:159], v240\n\tv_exp_f32 v130, v130\n\t" \
    "v_exp_f32 v131, v131\n\tv_exp_f32 v132, v132\n\tv_exp_f32 v133, v133\n\tv_mfma_f32_32x32x16_bf16 v[144:159], v[216:219], v[212:215], v[192:207]\n\t" \
    "v_exp_f32 v134, v134\n\tv_exp_f32 v135, v135\n\tv_exp_f32 v136, v136\n\tv_exp_f32 v137, v137\n\t" \
    "v_exp_f32 v138, v138\n\ts_waitcnt lgkmcnt(0)\n\tv_smfmac_f32_32x32x32_bf16 v[160:175], v[236:239], v[128:135], v240\n\tds_read_b128 v[220:223], v243 offset:512\n\t" \
    "ds_read_b128 v[224:227], v243\n\tv_exp_f32 v139, v139\n\tv_exp_f32 v140, v140\n\tv_exp_f32 v141, v141\n\t" \
    "v_exp_f32 v142, v142\n\tv_exp_f32 v143, v143\n\tv_exp_f32 v144, v144\n\tv_exp_f32 v145, v145\n\t" \
    "v_smfmac_f32_32x32x32_bf16 v[160:175], v[232:235], v[136:143], v240\n\tv_exp_f32 v146, v146\n\tv_exp_f32 v147, v147\n\tv_exp_f32 v148, v148\n\t" \
    "v_exp_f32 v149, v149\n\tv_mfma_f32_32x32x16_bf16 v[128:143], v[228:231], v[208:211], v[192:207]\n\tv_exp_f32 v150, v150\n\tv_exp_f32 v151, v151\n\t" \
    "v_exp_f32 v152, v152\n\tv_exp_f32 v153, v153\n\tv_exp_f32 v154, v154\n\tv_smfmac_f32_32x32x32_bf16 v[176:191], v[236:239], v[144:151], v240\n\t" \
    "ds_read_b128 v[216:219], v241 offset:33792\n\tv_exp_f32 v155, v155\n\tv_exp_f32 v156, v156\n\tv_exp_f32 v157, v157\n\t" \
    "v_exp_f32 v158, v158\n\tv_exp_f32 v159, v159\n\t" \
    ""
#define AT_ASM(TXT) asm volatile(TXT : "+{v[128:143]}"(sa), "+{v[144:159]}"(sb), "+{v[160:175]}"(oa), "+{v[176:191]}"(ob), "+{v[216:219]}"(k0), "+{v[220:223]}"(v10), "+{v[224:227]}"(v00), "+{v[228:231]}"(k1), "+{v[232:235]}"(v11), "+{v[236:239]}"(v01) \
    : "{v[192:207]}"(nega), "{v[208:211]}"(qfa), "{v[212:215]}"(qfb), "{v240}"(sidx), "{v241}"(ka), "{v242}"(va), "{v243}"(vnx) : "memory")

__device__ __forceinline__ unsigned geglu2(float g0, float u0, float g1, float u1, float ca, float cb, float ir2) {
  const float z0 = g0 * __builtin_fmaf(g0 * g0, cb, ca), z1 = g1 * __builtin_fmaf(g1 * g1, cb, ca);
  const float e0 = __builtin_amdgcn_exp2f(z0), e1 = __builtin_amdgcn_exp2f(z1);
  const float r0 = __builtin_amdgcn_rcpf(__builtin_fmaf(e0, ir2, ir2)), r1 = __builtin_amdgcn_rcpf(__builtin_fmaf(e1, ir2, ir2));
  return pk2((g0 * u0) * r0, (g1 * u1) * r1);
}

__global__ __launch_bounds__(512, 2) void k_block(const uint16_t* __restrict__ x, const uint16_t* __restrict__ wqkv, const uint16_t* __restrict__ wo, const uint16_t* __restrict__ wgate,
                                                  const uint16_t* __restrict__ wup, const uint16_t* __restrict__ wdn, char* __restrict__ ws, uint16_t* __restrict__ out) {
  extern __shared__ __attribute__((aligned(16))) char smem[];
  lptr lds = (lptr)smem;
  lptr ctl = lds + LDS_CTL;
  const int lane = threadIdx.x & 63, w = __builtin_amdgcn_readfirstlane(threadIdx.x >> 6);
  const int bid = blockIdx.x, xcd = bid & 7, bi = bid >> 3;
  const int t = lane & 31, h = lane >> 5;
  unsigned l16 = lane * 16;
  const rsrc_t rws = __builtin_amdgcn_make_buffer_rsrc(ws, 0, WS_END, 0x00020000);
  char* sync = ws + WS_SYNC;
  STAMP_DECL;
  if (w == 0) STAMP(0);
  const int q4 = (lane & 15) >> 2, p4 = lane & 3, nh = (lane >> 4) & 1;
  u64 tag;
#define TAKE_TAG() do { const u32x2 ev = *(LAS u32x2*)(ctl + 256); \
    tag = (((u64)(unsigned)__builtin_amdgcn_readfirstlane(ev[1]) << 32) | (unsigned)__builtin_amdgcn_readfirstlane(ev[0])) + 1; } while (0)

  bf16x8 f[8]; u32x2 cp[2]; bool last = false;
  unsigned rdv = 0, cd0, cd1;
  {
    const int rb = (xcd & 3) * 8 + (bi & 7), cb = (xcd >> 2) * 4 + (bi >> 3);
    const int m = w & 1, g = (w >> 1) & 1;
    LAS float* p_sum = (LAS float*)(lds + P1_SCR);
    LAS float* p_sq = p_sum + 128;
    LAS float* p_c = p_sq + 128;
    const unsigned xrow = (unsigned)((m * 32 + t) * 128), xsw = (unsigned)((h ^ ((t >> 1) & 7)) << 4);
    const unsigned wbase = (unsigned)(8192 + (8 * h + q4) * 384 + nh * 32 + p4 * 8 + ((g ^ (q4 >> 1)) << 6));
    const unsigned xl = (unsigned)((lane >> 3) * 1024 + (((lane & 7) ^ (lane >> 4)) << 4));
    unsigned wl[3];
#pragma unroll
    for (int v = 0; v < 3; ++v) { const int off = v * 1024 + lane * 16; const int r8 = (off * 171) >> 16, rem = off - r8 * 384; const int seg = (rem >> 6) ^ ((r8 >> 1) & 1);
      wl[v] = (unsigned)(r8 * 3072 + (seg >> 1) * 1024 + ((cb ^ (seg >> 1)) << 7) + (seg & 1) * 64 + (rem & 63) - v * 1024); }
    const uint16_t* xs = x + rb * 64 * DM; const uint16_t* wsrc = wqkv;
    const int rot = ROT == 2 ? ((bi ^ (bi >> 3)) & 1) * 4 : ROT == 4 ? ((bi + (bi >> 3)) & 3) * 2 : 0;
#define P1_X(c, p, xv) dma16(xv, xs + (p) * 8 * DM + (((c) + rot) & 7) * 64, (unsigned)(((c) & 3) * P1_SLOT + (p) * 1024))
#define P1_W(c, p) dma3v(wl[0], wl[1], wl[2], wsrc + ((((c) + rot) & 7) * 64 + (p) * 8) * 1536, (unsigned)(((c) & 3) * P1_SLOT + 8192 + (p) * 3072))
    P1_X(0, w, xl ^ ((w & 1) * 64)); P1_W(0, w);
    u32x4 selv; { const unsigned v = ((lane & 15) == nh) ? 0x3f803f80u : 0u; selv = (u32x4){v, v, v, v}; }
    const bf16x8 sel = as_frag(selv);
    if (w < 4) {
      const int lw = w;
      const unsigned xl1 = xl ^ 64u;
#define P1_DMA(c, i) do { if ((i) == 0) P1_X(c, lw * 2, xl); else if ((i) == 1) P1_X(c, lw * 2 + 1, xl1); else if ((i) == 2) P1_W(c, lw * 2); else if ((i) == 5) P1_W(c, lw * 2 + 1); } while (0)
      asm volatile("s_mov_b32 m0, %2\n\ts_nop 0\n\tglobal_load_lds_dword %0, %1 sc1" :: "v"((unsigned)(lane * 4)), "s"(sync + SY_EPOCH), "s"((unsigned)(LDS_CTL + 256)) : "memory");
#pragma unroll
      for (int i = 0; i < 8; ++i) P1_DMA(1, i);
      const unsigned xa0 = xrow + (xsw ^ (g * 32)), xa1 = xrow + (xsw ^ (g * 32) ^ 64);
      float s1 = 0.f, s2 = 0.f;
      const unsigned stg = lw * 8192;
      unsigned rvo = 0; const uint16_t* rs0 = wgate; const uint16_t* rs1 = wup; unsigned rstep = 8 * FF;
      if (bid < 112) rvo = (unsigned)((((bid & 3) * 128 + lw * 32 + (lane >> 3)) * FF + (bid >> 2) * 64) * 2 + (lane & 7) * 16);
      else if (bid < 184) { const int u = bid - 112; const bool isd = u >= 16; const int e = isd ? u - 16 : u;
        rvo = (unsigned)((((e >> 3) * 256 + lw * 64 + (lane >> 3)) * DM + (e & 7) * 64) * 2 + (lane & 7) * 16); rs0 = isd ? wdn : wo; rs1 = rs0 + 32 * DM; rstep = 8 * DM; }
#pragma unroll
      for (int c = 0; c < 8; ++c) {
        if (c == 0) WAITV(9); else if (c < 6) WAITV(16); else if (c == 6) WAITV(16); else WAITV(8);
        BARRIER();
        lptr sb = lds + (c & 3) * P1_SLOT;
        const u32x4 x0 = *(LAS u32x4*)(sb + xa0), x1 = *(LAS u32x4*)(sb + xa1);
        SBAR();
#define P1_NEXT(i) do { if (c == 0) { P1_DMA(2 + (i) / 4, ((i) & 3) * 2); P1_DMA(2 + (i) / 4, ((i) & 3) * 2 + 1); } else if (c < 5) P1_DMA(c + 3, i); \
          else if (c == 5) dma16(rvo, ((i) & 1 ? rs1 : rs0) + ((i) >> 1) * rstep, stg + ((i) & 1) * 4096 + ((i) >> 1) * 1024); SBAR(); } while (0)
#pragma unroll
        for (int i = 0; i < 8; ++i) { P1_NEXT(i);
          const unsigned d = i < 4 ? x0[i & 3] : x1[i & 3]; const float lo = bflo(d), hi = bfhi(d);
          s1 = (s1 + lo) + hi; s2 = __builtin_fmaf(hi, hi, __builtin_fmaf(lo, lo, s2)); asm volatile("" : "+v"(s1), "+v"(s2)); SBAR(); }
        if (c == 1) TAKE_TAG();
      }
      s1 = swap32_add(s1); s2 = swap32_add(s2);
      if (lane < 32) { p_sum[g * 64 + m * 32 + t] = s1; p_sq[g * 64 + m * 32 + t] = s2; }
      lds_signal(ctl + C_ST, lane);
      if (w == 0) STAMP(23);
      WAITV(0);
      if (bid < 184) {
        const bool gu = bid < 112;
        lptr rp = lds + stg + (4 * h + q4) * 128 + p4 * 8 + nh * (gu ? 4096 : 32);
#pragma unroll
        for (int i = 0; i < 8; ++i) { lptr q = rp + (gu ? (i & 1) * 2048 + (i >> 1) * 32 : (i & 3) * 2048 + (i >> 2) * 64); f[i] = join(lds_tr(q), lds_tr(q + 1024)); }
        if (gu) {
          LAS float* cs = (LAS float*)(lds + P1_SCR + 8192);
#pragma unroll
          for (int i = 0; i < 4; ++i) { const f32x4 c = MFMA16(f[2 * i + 1], sel, MFMA16(f[2 * i], sel, (f32x4){}));
            if ((lane & 15) < 2) *(LAS f32x4*)(cs + lw * 128 + i * 32 + (lane & 15) * 16 + (lane >> 4) * 4) = c; }
          last = lds_arrive(ctl + C_RT, lane) == 3;
          if (last) {
#pragma unroll
            for (int r = 0; r < 2; ++r) { const int n = r * 64 + lane; const float c = (cs[n] + cs[128 + n]) + (cs[256 + n] + cs[384 + n]);
              cp[r] = (u32x2){pk2(c, c), pk2(c - bfhi(pk2(c, c)), c - bfhi(pk2(c, c)))}; } }
        }
      }
      {
        const bool gu = bid < 112; const int u = bid - 112; const bool isd = u >= 16; const int e = isd ? u - 16 : u;
        unsigned c32[4];
#pragma unroll
        for (int i = 0; i < 4; ++i) {
          const int nt = (bid >> 2) * 4 + i, cbk = nt / 7, ti = nt - cbk * 7;
          const unsigned b0 = ti < 3 ? ti : ti < 5 ? 96 + ti : 160 + ti, st = ti < 3 ? 3 : 2, gd = WS_WG + ((cbk * 231 + b0 + st * ((bid & 3) * 8 + lw * 2)) << 10);
          c32[i] = WS_WG + ((cbk * 231 + b0 + st * 32) << 10) + (bid & 2) * 256 + (bid & 1) * 8;
          const int k0 = (e >> 3) * 16 + lw * 4 + ((2 * i) & 3), k1 = k0 + 1;
          const unsigned ks_n = isd ? 112 : 32, pd = (isd ? WS_WD : WS_WO) + ((((e & 7) * 2 + (i >> 1)) * ks_n) << 10);
          const unsigned d0 = bid >= 184 ? WS_END : gu ? gd : pd + ((isd ? p5_pos(k0 / 7, k0 % 7) : k0) << 10);
          const unsigned d1 = bid >= 184 ? WS_END : gu ? gd + (st << 10) : pd + ((isd ? p5_pos(k1 / 7, k1 % 7) : k1) << 10);
          rdv = lane == 2 * i ? d0 : lane == 2 * i + 1 ? d1 : rdv;
        }
        PIN(rdv);
        cd0 = last ? (h ? c32[1] : c32[0]) + t * 16 : WS_END; cd1 = last ? (h ? c32[3] : c32[2]) + t * 16 : WS_END;
      }
    } else {
      if (w == 4 && lane < 32) *(LAS unsigned*)(ctl + lane * 4) = 0u;
      __builtin_amdgcn_s_setprio(1);
      if (w == 5 && lane < 32) *(LAS u32x4*)(lds + AT_ONE + (lane >> 1) * 512 + (lane & 1) * 16) = (lane & 1) ? (u32x4){0u, 0u, 0u, 0u} : (u32x4){0x3f803f80u, 0x3f803f80u, 0x3f803f80u, 0x3f803f80u};
      unsigned xa[4];
#pragma unroll
      for (int s = 0; s < 4; ++s) xa[s] = xrow + (xsw ^ ((s ^ m) * 32));
      const unsigned wa[2] = {wbase + m * 6144, wbase - m * 6144};
      {
        u64 pc; asm volatile("s_getpc_b64 %0" : "=s"(pc));
        asm volatile("s_mov_b32 m0, %2\n\ts_nop 0\n\tglobal_load_lds_dword %0, %1" :: "v"((unsigned)(min(bi * 4 + (lane & 3) + (w - 4) * 128, CODE_LINES - 1) * 128)), "s"(pc & ~127ull), "s"((unsigned)(LDS_CTL + 512)) : "memory");
      }
      if (P1T) {
        const int cw = w - 4;
        const unsigned vo = cw < 3 ? (unsigned)((256 + t * 8 + (bi & 7)) * 3072 + cw * 1024 + ((cb ^ cw) << 7)) : (unsigned)(((lane >> 2) * 4 + (bi >> 3)) * 1024 + (4 + (lane & 3)) * 128);
        asm volatile("s_mov_b32 m0, %2\n\ts_nop 0\n\tglobal_load_lds_dword %0, %1" :: "v"(vo), "s"(cw < 3 ? wqkv : xs), "s"((unsigned)(LDS_CTL + 512)) : "memory");
      }
      f32x16 aq = {}, ak = {}, av = {}; f32x4 cq = {}, ck = {}, cv = {};
      bf16x8 xf, wq, wk, wv;
#define P1_LX(sb, s) lds_frag(lds + (sb) + xa[s])
#define P1_LW(sb, s, o) join(lds_tr(lds + (sb) + wa[(s) & 1] + (s) * 6144 + (o)), lds_tr(lds + (sb) + wa[(s) & 1] + (s) * 6144 + (o) + 1536))
      WAITV(1 + P1T); BARRIER();
      if (w == 4) STAMP(1);
      xf = P1_LX(0, 0); wk = P1_LW(0, 0, 128); wv = P1_LW(0, 0, 256); wq = P1_LW(0, 0, 0);
#pragma unroll
      for (int ks = 0; ks < 32; ++ks) {
        const int sn = (((ks + 1) >> 2) & 3) * P1_SLOT, s1 = (ks + 1) & 3;
        const bf16x8 x0 = xf;
        if (s1 == 0 && ks < 31) BARRIER();
        SBAR();
        ak = MFMA32(wk, x0, ak); if (!(ks & 1)) ck = MFMA16(wk, sel, ck); if (ks < 31) { xf = P1_LX(sn, s1); wk = P1_LW(sn, s1, 128); } SBAR();
        av = MFMA32(x0, wv, av); if (!(ks & 1)) cv = MFMA16(wv, sel, cv); if (ks < 31) wv = P1_LW(sn, s1, 256); SBAR();
        aq = MFMA32(wq, x0, aq); if (!(ks & 1)) cq = MFMA16(wq, sel, cq); if (ks < 31) wq = P1_LW(sn, s1, 0);
        PIN(ak); PIN(av); PIN(aq); SBAR();
      }
      if ((lane & 15) < 2) { const int n0 = g * 32 + (lane & 15) * 16 + (lane >> 4) * 4;
        *(LAS f32x4*)(p_c + m * 192 + n0) = cq; *(LAS f32x4*)(p_c + m * 192 + 64 + n0) = ck; *(LAS f32x4*)(p_c + m * 192 + 128 + n0) = cv; }
      lds_signal(ctl + C_ST, lane);
      if (w == 4) STAMP(2);
      TAKE_TAG();
      lds_wait(ctl + C_ST, 8);
      if (w == 4) STAMP(22);
      const float mean = (p_sum[m * 32 + t] + p_sum[64 + m * 32 + t]) * (1.f / DM);
      const float rstd = __builtin_amdgcn_rsqf(fmaxf((p_sq[m * 32 + t] + p_sq[64 + m * 32 + t]) * (1.f / DM) - mean * mean, 0.f) + LN_EPS);
      LAS float* st_w = p_c + 384 + (w - 4) * 32;
      if (lane < 32) st_w[t] = rstd;
      {
        const unsigned mm = pk2(-mean, -mean - bfhi(pk2(-mean, -mean)));
        const bf16x8 mf = as_frag((u32x4){mm, mm, 0u, 0u});
        const float c0 = p_c[h * 192 + g * 32 + t], c1 = p_c[h * 192 + 64 + g * 32 + t], c2 = p_c[h * 192 + 128 + g * 32 + t];
#define CFRAG(c) as_frag((u32x4){pk2(c, c), pk2(c - bfhi(pk2(c, c)), c - bfhi(pk2(c, c))), 0u, 0u})
        ak = MFMA32(CFRAG(c1), mf, ak); av = MFMA32(mf, CFRAG(c2), av); aq = MFMA32(CFRAG(c0), mf, aq);
      }
      const int mt = rb * 2 + m, head0 = cb * 4 + g * 2, headk = head0 ^ 4, headv = head0 ^ 8;
      u32x4 o[2]; float n2[2] = {0.f, 0.f};
#pragma unroll
      for (int j = 0; j < 8; ++j) { const float a = ak[2 * j] * rstd, b = ak[2 * j + 1] * rstd; o[j >> 2][j & 3] = pk2(a, b); n2[j >> 2] += a * a + b * b; }
      const int rho = (t & 19) | ((t & 4) << 1) | ((t & 8) >> 1);
#pragma unroll
      for (int i = 0; i < 2; ++i) stc16(rws, (h * 32 + rho) * 16, WS_K + (((headk + i) * 64 + mt) << 10), o[i]);
#pragma unroll
      for (int qd = 0; qd < 4; ++qd) { const f32x4 rs = *(LAS f32x4*)(st_w + qd * 8 + h * 4);
        o[qd >> 1][(qd & 1) * 2] = pk2(av[qd * 4] * rs[0], av[qd * 4 + 1] * rs[1]); o[qd >> 1][(qd & 1) * 2 + 1] = pk2(av[qd * 4 + 2] * rs[2], av[qd * 4 + 3] * rs[3]); }
#pragma unroll
      for (int i = 0; i < 2; ++i) stc16(rws, (t >> 4) * 65536 + h * 256 + (t & 15) * 16, WS_V + ((headv * 64 + mt) << 10) + i * 512, o[i]);
      {
        auto r = __builtin_amdgcn_permlane32_swap(__float_as_uint(n2[0]), __float_as_uint(n2[1]), false, false);
        int x = __float_as_int(__uint_as_float(r[0]) + __uint_as_float(r[1])) & 0x7fffffff;
        x = max(x, __builtin_amdgcn_update_dpp(0, x, 0x128, 0xf, 0xf, false)); x = max(x, __builtin_amdgcn_update_dpp(0, x, 0x124, 0xf, 0xf, false));
        x = max(x, __builtin_amdgcn_update_dpp(0, x, 0x122, 0xf, 0xf, false)); x = max(x, __builtin_amdgcn_update_dpp(0, x, 0x121, 0xf, 0xf, false));
        if ((lane & 15) == 0) stc4(rws, (unsigned)(h * 512 + nh * 4), WS_KMAX + (headk * 64 + mt) * 8, (unsigned)x);
      }
      const float sc = rstd * QSCALE;
#pragma unroll
      for (int j = 0; j < 8; ++j) o[j >> 2][j & 3] = pk2(aq[2 * j] * sc, aq[2 * j + 1] * sc);
#pragma unroll
      for (int i = 0; i < 2; ++i) stc16(rws, l16, WS_Q + (((head0 + i) * 64 + mt) << 10), o[i]);
      if (w == 4) STAMP(3);
      WAITV(0);
      if (lds_arrive(ctl + C_F1, lane) == 3) flag_set(sync + SY_F1 + (cb * 32 + rb) * 128, tag, lane);
      __builtin_amdgcn_s_setprio(0);
#pragma unroll
      for (int j = 0; j < 8; ++j) asm volatile("" : "=v"(f[j]));
      asm volatile("" : "=v"(cp[0]), "=v"(cp[1]), "=v"(cd0), "=v"(cd1), "=v"(rdv));
      if (w == 4) { STAMP(4);
        char* fq = sync + SY_F1 + (xcd * 32 + (bi >> 2) * 4 + (lane & 3)) * 128;
        if (DLY) __builtin_amdgcn_s_sleep(DLY);
        poll2x(fq, sync + SY_F1 + ((xcd ^ (1 + h)) * 32 + t) * 128, tag); }
    }
  }
  BARRIER();
  if (w == 0) STAMP(5);

  const int rb3 = xcd * 4 + (bi & 3), cb3 = bi >> 2;
  const int rb4 = (xcd >> 1) * 4 + (bi & 3), cb4 = (xcd & 1) * 8 + (bi >> 2);
  const int m3 = (w >> 1) & 1, n3 = w & 1, kh = w >> 2;
  const char* gsrc = ws + WS_WG + ((size_t)(cb4 * 231) << 10);
  u32x2 rx[2]; bf16x8 wof[16];
  bf16x8 wdr[40];
  const unsigned wd_src = WS_WD + (unsigned)(((cb3 * 2 + (w & 1)) * 112 + ((w >> 1) & 1) * 56) << 10);
#define WDR(a, b) do { _Pragma("unroll") for (int j = (a); j < (b); ++j) wdr[j] = as_frag(ldc16(rws, l16, wd_src + ((j < 16 ? j : j + 16) << 10))); } while (0)
  {
    const int head = xcd * 4 + (bi & 3), qb = bi >> 2, mt = qb * 8 + (w & 3) * 2;
    const char* kg = ws + WS_K + ((size_t)head << 16);
    u32x4 qa, qb_; u32x2 km;
    asm volatile("global_load_dwordx4 %0, %1, %2 sc1" : "=v"(qa) : "v"(l16), "s"(ws + WS_Q + ((size_t)(head * 64 + mt) << 10)) : "memory");
    asm volatile("global_load_dwordx4 %0, %1, %2 offset:1024 sc1" : "=v"(qb_) : "v"(l16), "s"(ws + WS_Q + ((size_t)(head * 64 + mt) << 10)) : "memory");
    asm volatile("global_load_dwordx2 %0, %1, %2 sc1" : "=v"(km) : "v"((unsigned)(lane * 8)), "s"(ws + WS_KMAX + head * 512) : "memory");
#define KV_DMA(c, j, tile) dma16c(l16, kg + ((j) & 1) * (WS_V - WS_K) + (((j) >> 1) * 32 + (c) * 8 + (tile)) * 1024, (unsigned)((c) * AT_CH + (j) * 8192 + (tile) * 1024))
#define OPAQUE_W(n) int n = w; asm volatile("" : "+s"(n))
#define KV_PAIR(c) do { OPAQUE_W(wo_); _Pragma("unroll") for (int e = 0; e < 2; ++e) \
      dma4c(l16, kg + (wo_ & 1) * (WS_V - WS_K) + ((wo_ >> 1) * 32 + (c) * 8 + e * 4) * 1024, (unsigned)((c) * AT_CH + wo_ * 8192 + e * 4096)); } while (0)
#pragma unroll
    for (int j = 0; j < 4; ++j) KV_DMA(0, j, w);
    asm volatile("s_waitcnt vmcnt(4)" : "+v"(qa), "+v"(qb_), "+v"(km) :: "memory");
    const bf16x8 qfa = as_frag(qa), qfb = as_frag(qb_);
    float na = 0.f, nb = 0.f;
#pragma unroll
    for (int j = 0; j < 4; ++j) { na += bflo(qa[j]) * bflo(qa[j]) + bfhi(qa[j]) * bfhi(qa[j]); nb += bflo(qb_[j]) * bflo(qb_[j]) + bfhi(qb_[j]) * bfhi(qb_[j]); }
    const float kn = wave_max_nonneg(__uint_as_float(max(km[0], km[1])));
    float ma = __builtin_amdgcn_sqrtf(swap32_add(na) * kn) * 1.01f + 1e-6f, mb = __builtin_amdgcn_sqrtf(swap32_add(nb) * kn) * 1.01f + 1e-6f;
    const bool fast = __all(ma <= SAFE_BOUND && mb <= SAFE_BOUND);
    unsigned ka = l16 + kh * 16384;
    unsigned va = (t < 16) ? (unsigned)(kh * 16384 + 8192 + h * 256 + t * 16) : (unsigned)(AT_ONE + ((t == 16 || t == 20) ? 0 : 16));
    const unsigned vstep = (t < 16) ? (unsigned)AT_CH : 0u;
    f32x16 oa = {}, ob = {};
#define KV_HALF(c, e) dma4c(l16, kg + (w & 1) * (WS_V - WS_K) + ((w >> 1) * 32 + (c) * 8 + (e) * 4) * 1024, (unsigned)((c) * AT_CH + w * 8192 + (e) * 4096))
#define RT_STORE(j) stc16(rws, l16, __builtin_amdgcn_readlane(rdv, j), __builtin_bit_cast(u32x4, f[j]))
    auto p3_operands = [&]() __attribute__((always_inline)) {
      const unsigned wo_src = WS_WO + (unsigned)(((cb3 * 2 + n3) * 32 + kh * 16) << 10);
#pragma unroll
      for (int i = 0; i < 16; ++i) wof[i] = as_frag(ldc16(rws, l16, wo_src + i * 1024));
      unsigned lq = l16; asm volatile("" : "+v"(lq));
      const unsigned xo = (((rb3 * 2 + m3) * 32 + ((lq >> 4) & 31)) * DM + (cb3 * 2 + n3) * 32 + kh * 16) * 2 + (lq >> 9) * 8;
      rx[0] = *(const u32x2*)((const char*)x + xo); rx[1] = *(const u32x2*)((const char*)x + xo + 16);
    };
    auto hook = [&](auto C, auto I) __attribute__((always_inline)) { constexpr int c = decltype(C)::v, i = decltype(I)::v;
      if constexpr (c == 0) {
        if constexpr (i == 2) { WAITV(0); lds_signal(ctl + C_L1, (int)l16); }
        if constexpr (i >= 2 && i < 6) KV_HALF(1 + i / 2, i & 1);
        if constexpr (i >= 6) { RT_STORE(2 * i - 12); RT_STORE(2 * i - 11); }
      } else if constexpr (c == 1) {
        if constexpr (i == 0) { RT_STORE(4); RT_STORE(5); }
        if constexpr (i == 1) { RT_STORE(6); RT_STORE(7); stc8(rws, cd0, 0, cp[0]); stc8(rws, cd1, 0, cp[1]); WAITV(10); lds_signal(ctl + C_L2, (int)l16); }
        if constexpr (i == 4) { WAITV(0); if (lds_arrive(ctl + C_RS, (int)l16) == 3) flag_set(sync + SY_FW + bid * 128, tag, (int)l16); }
        if constexpr (i == 7) __builtin_amdgcn_s_setprio(2);
      } else if constexpr (c == 2) {
        if constexpr (i == 0) { if (w == 0) { RELANE(); poll3x(sync + SY_FW + lane * 128, sync + SY_FW + (64 + lane) * 128, sync + SY_FW + (128 + min(lane, 55)) * 128, tag); lds_signal(ctl + C_FW, (int)l16); } }
        if constexpr (i == 1) lds_wait_slow(ctl + C_FW, 1);
        if constexpr (i >= 1 && i <= NWD / 4) WDR(i * 4 - 4, i * 4);
        if constexpr (i == 2 && TCH) { RELANE(); TOUCH(gsrc + (bi & 3) * 59136 + w * 8192, lane); }
        if constexpr (i == 4 && TCH) { RELANE(); TOUCH(gsrc + (bi & 3) * 59136, min(256 + w * 64 + lane, 461)); }
        if constexpr (i == 6 && TCH) { RELANE(); TOUCH(ws + WS_WO + ((cb3 * 64 + (bi & 3) * 16) << 10) + w * 4096, lane >> 1); }
        if constexpr (i == 7) { if (TCH) { RELANE(); TOUCH(ws + wd_src + (16 + (bi & 3) * 4) * 1024, lane + h * 160); } __builtin_amdgcn_s_setprio(1); lds_signal(ctl + C_D3, (int)l16); }
      } else {
        if constexpr (i == 1) lds_wait_slow(ctl + C_D3, 8);
        if constexpr (i >= 1 && i < 7) dma4c(l16, gsrc + (w * 24 + (i - 1) * 4) * 1024, (unsigned)((w * 24 + (i - 1) * 4) * 1024));
      }
    };
    PIN(l16);
    __builtin_amdgcn_s_setprio(3);
    WAITV(0); BARRIER();
    if (w == 4) STAMP(35);
    if (w < 4) { KV_HALF(1, 0); KV_HALF(1, 1); }
    if (w == 0) STAMP(6);
    int path = fast ? (w < 4 ? 2 : 1) : 0;
#define AT_BEGIN() ma = mb = fmaxf(ma, mb);         \
      f32x16 nega; _Pragma("unroll") for (int r = 0; r < 16; ++r) nega[r] = -ma; \
      bf16x8 k1 = lds_frag(lds + ka), v11 = k1, k0 = lds_frag(lds + ka + 1024), v10 = lds_frag(lds + va + 512), v00 = lds_frag(lds + va), v01 = v00; \
      f32x16 sa = MFMA32(k1, qfa, nega), sb = {}; const unsigned sidx = 0xDDDDDDDDu; \
      asm volatile("s_nop 7\n\ts_nop 3" : "+v"(sa), "+v"(k0), "+v"(v10), "+v"(v00))
#define AT_END() asm volatile("s_waitcnt lgkmcnt(0)\n\ts_nop 1" : "+v"(k0), "+v"(k1), "+v"(v10), "+v"(v00), "+v"(sb));        \
      ob = SMF(v11, hi8(sb), ob); asm volatile("s_nop 7\n\ts_nop 3" : "+v"(oa))
    asm volatile("" : "+s"(path));
    if (path == 0) {
      ma = -INFINITY; mb = -INFINITY;
      sfor<0, 4>([&](auto C) __attribute__((always_inline)) { constexpr int c = decltype(C)::v;
        if (c == 1 || c == 2) lds_wait(ctl + C_L1 + (c - 1) * 4, 4);
        if (w < 4) { if constexpr (c == 3) lds_signal(ctl + C_D3, (int)l16);
          sfor<0, 8>([&](auto I) __attribute__((always_inline)) { if constexpr (!(c == 2 && decltype(I)::v == 7)) hook(C, I); }); }
        else if (c == 3) lds_signal(ctl + C_D3, (int)l16);
#pragma unroll 1
        for (int i = 0; i < 8; ++i) {
          const bf16x8 kf = lds_frag(lds + ka + i * 1024);
          const bf16x8 v0 = lds_frag(lds + va + i * 1024), v1 = lds_frag(lds + va + i * 1024 + 512);
#define AT_SLOW(Q, M, O) do { f32x16 s = MFMA32(kf, Q, (f32x16){}); float mx = s[0]; \
          _Pragma("unroll") for (int r = 1; r < 16; ++r) mx = fmaxf(mx, s[r]); \
          mx = fmaxf(swap32_max(mx), M); const float al = __builtin_amdgcn_exp2f(M - mx); M = mx; \
          _Pragma("unroll") for (int r = 0; r < 16; ++r) { O[r] *= al; s[r] = __builtin_amdgcn_exp2f(s[r] - mx); } \
          O = SMF(v0, lo8(s), O); O = SMF(v1, hi8(s), O); } while (0)
          AT_SLOW(qfa, ma, oa); AT_SLOW(qfb, mb, ob);
        }
        ka += AT_CH; va += vstep; });
      if (YEARLY || w < 4) { lds_wait_slow(ctl + C_FW, 1); p3_operands(); }
#pragma unroll
        for (int j = 0; j < 8; ++j) asm volatile("" : "=v"(f[j]));
        asm volatile("" : "=v"(cp[0]), "=v"(cp[1]), "=v"(cd0), "=v"(cd1), "=v"(rdv));
    }
    asm volatile("" : "+s"(path));
    if (path == 1) {
      AT_BEGIN();
#pragma unroll 1
      for (int c = 0; c < 4; ++c) {
        if (c == 2) __builtin_amdgcn_s_setprio(2);
        if (c == 3) { __builtin_amdgcn_s_setprio(1); lds_signal(ctl + C_D3, (int)l16); }
        const unsigned vnx = va + (c < 3 ? vstep : 0u);
        AT_ASM(AT_TXT0 AT_TXT1 AT_TXT2 AT_TXT3 AT_TXT4 AT_TXT5);
        if (c < 2) lds_wait(ctl + C_L1 + c * 4, 4); else if (c == 3) __builtin_amdgcn_s_setprio(0);
        AT_ASM(AT_TXT6 AT_TXT7);
        ka += AT_CH; va = vnx;
      }
      AT_END();
#pragma unroll
      for (int j = 0; j < NWD; ++j) asm volatile("" : "=v"(wdr[j]));
#pragma unroll
      for (int j = 0; j < 16; ++j) asm volatile("" : "=v"(wof[j]));
      asm volatile("" : "=v"(rx[0]), "=v"(rx[1]));
#pragma unroll
      for (int j = 0; j < 8; ++j) asm volatile("" : "=v"(f[j]));
      asm volatile("" : "=v"(cp[0]), "=v"(cp[1]), "=v"(cd0), "=v"(cd1), "=v"(rdv));
    }
    asm volatile("" : "+s"(path));
    if (path == 2) {
      AT_BEGIN();
      sfor<0, 4>([&](auto C) __attribute__((always_inline)) { constexpr int c = decltype(C)::v;
        const unsigned vnx = va + (c < 3 ? vstep : 0u);
        AT_ASM(AT_TXT0); hook(C, IC<0>{}); AT_ASM(AT_TXT1); hook(C, IC<1>{}); AT_ASM(AT_TXT2); hook(C, IC<2>{});
        AT_ASM(AT_TXT3); hook(C, IC<3>{}); AT_ASM(AT_TXT4); hook(C, IC<4>{}); AT_ASM(AT_TXT5); hook(C, IC<5>{});
        if (c < 2) lds_wait(ctl + C_L1 + c * 4, 4);
        AT_ASM(AT_TXT6); hook(C, IC<6>{}); AT_ASM(AT_TXT7); hook(C, IC<7>{});
        ka += AT_CH; va = vnx; });
      AT_END();
      if (w == 0) STAMP(34);
      p3_operands();
    }
    __builtin_amdgcn_s_setprio(0);
    if (w == 0) STAMP(7);
    if (w == 4) STAMP(33);
    unsigned l8 = l16; asm volatile("" : "+v"(l8)); l8 >>= 1;
    { lptr xp = lds + AT_X + w * 2560 + l16; const f32x16& s = kh ? oa : ob;
      *(LAS f32x4*)xp = (f32x4){s[0], s[1], s[2], s[3]}; *(LAS f32x4*)(xp + 1024) = (f32x4){s[4], s[5], s[6], s[7]};
      *(LAS f32x2*)(lds + AT_X + w * 2560 + 2048 + l8) = (f32x2){s[8], kh ? ma : mb}; }
    BARRIER();
    if (w == 0) STAMP(21);
    { lptr xp = lds + AT_X + (w ^ 4) * 2560 + l16; const f32x16& s = kh ? ob : oa; const float mo = kh ? mb : ma;
      const f32x4 r0 = *(LAS f32x4*)xp, r1 = *(LAS f32x4*)(xp + 1024); const f32x2 r2 = *(LAS f32x2*)(lds + AT_X + (w ^ 4) * 2560 + 2048 + l8);
      const float mm = fmaxf(mo, r2[1]), f0 = __builtin_amdgcn_exp2f(mo - mm), f1 = __builtin_amdgcn_exp2f(r2[1] - mm);
      const float rl = __builtin_amdgcn_rcpf(s[8] * f0 + r2[0] * f1), g0 = f0 * rl, g1 = f1 * rl;
      const u32x4 ov = {pk2(s[0] * g0 + r0[0] * g1, s[1] * g0 + r0[1] * g1), pk2(s[2] * g0 + r0[2] * g1, s[3] * g0 + r0[3] * g1),
                        pk2(s[4] * g0 + r1[0] * g1, s[5] * g0 + r1[1] * g1), pk2(s[6] * g0 + r1[2] * g1, s[7] * g0 + r1[3] * g1)};
      stc16(rws, l16, WS_O + (((mt + kh) * 32 + head) << 10), ov); }
    WAITV(0); flag_set(sync + SY_F2 + ((mt + kh) * 32 + head) * 128, tag, (int)l16);
    if (!YEARLY && w >= 4) p3_operands();
    asm volatile("" : "+v"(wof[0]), "+v"(wof[1]), "+v"(wof[2]), "+v"(wof[3]), "+v"(wof[4]), "+v"(wof[5]), "+v"(wof[6]), "+v"(wof[7]), "+v"(wof[8]), "+v"(wof[9]), "+v"(wof[10]), "+v"(wof[11]),
                      "+v"(wof[12]), "+v"(wof[13]), "+v"(wof[14]), "+v"(wof[15]), "+v"(rx[0]), "+v"(rx[1]));
#pragma unroll
    for (int j = 0; j < NWD; ++j) PIN(wdr[j]);
    if (w == 0) { STAMP(8); if (DLY) __builtin_amdgcn_s_sleep(DLY); poll1(sync + SY_F2 + rb3 * 8192 + l8 * 16, tag); }
  }
  BARRIER();
  if (w == 0) STAMP(9);

  {
    RELANE();
    const char* osrc = ws + WS_O + ((size_t)((rb3 * 2 + (w & 1)) * 32) << 10);
    const unsigned odst = P3_O + ((w & 1) * 32 + ((w >> 1) & 1) * 16 + (w >> 2) * 4) * 1024;
#pragma unroll
    for (int r = 0; r < 2; ++r) dma4c(l16, osrc + ((((w >> 1) & 1) * 16 + (w >> 2) * 4 + r * 8) << 10), odst + r * 8192);
    f32x16 acc = {};
    lptr bp = lds + P3_O + (m3 * 32 + kh * 16) * 1024 + l16;
    WAITV(4); BARRIER();
#pragma unroll
    for (int s = 0; s < 8; ++s) acc = MFMA32(wof[s], lds_frag(bp + s * 1024), acc);
    WAITV(0); BARRIER();
#pragma unroll
    for (int s = 8; s < 16; ++s) acc = MFMA32(wof[s], lds_frag(bp + s * 1024), acc);
    BARRIER();
    if (w == 0) STAMP(10);
    { lptr xp = lds + P3_O + w * 2048 + l16; const f32x8 s = kh ? lo8(acc) : hi8(acc);
      *(LAS f32x4*)xp = __builtin_shufflevector(s, s, 0, 1, 2, 3); *(LAS f32x4*)(xp + 1024) = __builtin_shufflevector(s, s, 4, 5, 6, 7); }
    BARRIER();
    lptr xp = lds + P3_O + (w ^ 4) * 2048 + l16;
    const f32x4 r0 = *(LAS f32x4*)xp, r1 = *(LAS f32x4*)(xp + 1024);
    const f32x8 s = (kh ? hi8(acc) : lo8(acc)) + __builtin_shufflevector(r0, r1, 0, 1, 2, 3, 4, 5, 6, 7);
    u32x4 o; float s1 = 0.f, s2 = 0.f;
#pragma unroll
    for (int j = 0; j < 4; ++j) { const unsigned a = pk2(s[2 * j] + bflo(rx[j >> 1][j & 1]), s[2 * j + 1] + bfhi(rx[j >> 1][j & 1])); o[j] = a;
      s1 += bflo(a) + bfhi(a); s2 += bflo(a) * bflo(a) + bfhi(a) * bfhi(a); }
    const int mt = rb3 * 2 + m3, un = (cb3 * 2 + n3) * 2 + kh;
    stc16(rws, l16, WS_H + ((mt * 32 + un) << 10), o);
    s1 = swap32_add(s1); s2 = swap32_add(s2);
    { LAS f32x2* sp = (LAS f32x2*)(lds + P3_O + 16384) + m3 * 128;
      if (lane < 32) sp[(n3 * 2 + kh) * 32 + t] = (f32x2){s1, s2};
      if (lds_arrive(ctl + C_S3 + m3 * 4, (int)l16) == 3 && lane < 32) { const f32x2 v = (sp[t] + sp[32 + t]) + (sp[64 + t] + sp[96 + t]);
        stc8(rws, t * 8, WS_HST + (mt * 8 + cb3) * 256, (u32x2){__float_as_uint(v[0]), __float_as_uint(v[1])}); } }
    WAITV(0);
    if (lds_arrive(ctl + C_F3 + m3 * 4, (int)l16) == 3) flag_set(sync + SY_F3 + (mt * 8 + cb3) * 128, tag, (int)l16);
    if (w == 0) STAMP(11);
  }

  if (w >= 4) {
    const int cw = w - 4;
    RELANE();
    __builtin_amdgcn_s_setprio(2);
    if (DLY) __builtin_amdgcn_s_sleep(DLY);
    poll(sync + SY_F3 + (rb4 * 4 + cw) * 1024, 8, tag, lane);
    if (l16 == 0) __hip_atomic_store((LAS unsigned*)(ctl + C_H + cw * 4), 1u, __ATOMIC_RELAXED, __HIP_MEMORY_SCOPE_WORKGROUP);
    if (w == 4) STAMP(12);
    const int mt = rb4 * 4 + cw;
    bf16x8 hf[33];
#define HLOAD(s) hf[s] = as_frag(ldc16(rws, l16, WS_H + ((mt * 32 + (s)) << 10)))
#pragma unroll
    for (int s = 0; s < HWIN; ++s) HLOAD(s);
    SBAR();
    float ca, cbb, ve;
    const unsigned ab = WS_A + ((mt * 112 + p5_pos(cb4, 0)) << 10), ab3 = WS_A + ((mt * 112 + p5_pos(cb4, 3)) << 10), ab5 = WS_A + ((mt * 112 + p5_pos(cb4, 5)) << 10);
    unsigned lb0 = l16, lb1 = l16 + 65536, lb2 = l16 + 131072;
    asm volatile("" : "+v"(lb0), "+v"(lb1), "+v"(lb2));
    f32x16 a0 = {}, a1 = {}, a2 = {}, b0 = {}, b1 = {};
    u32x4 ov;
#define EPI(acc, q) ov[q] = geglu2(acc[2 * (q)], acc[8 + 2 * (q)], acc[2 * (q) + 1], acc[9 + 2 * (q)], ca, cbb, ve)
#define WF(stage, s, i) lds_frag(lds + (gu_slot(stage, s, i) < 64 ? lb0 : gu_slot(stage, s, i) < 128 ? lb1 : lb2) + (gu_slot(stage, s, i) & 63) * 1024)
    bf16x8 w0[2] = {WF(0, 0, 0), WF(0, 1, 0)}, w1[2] = {WF(0, 0, 1), WF(0, 1, 1)}, w2[2] = {WF(0, 0, 2), WF(0, 1, 2)};
    sfor<0, 33>([&](auto S) __attribute__((always_inline)) { constexpr int s = decltype(S)::v;
      if (s == 29 || s == 31) BARRIER();
      SBAR();
      a0 = MFMA32(w0[s & 1], hf[s], a0); PIN(a0); w0[s & 1] = s < 31 ? WF(0, s + 2, 0) : WF(1, (32 - s) ^ 1, 0); if constexpr (s + HWIN < 32) HLOAD(s + HWIN); SBAR();
      a1 = MFMA32(w1[s & 1], hf[s], a1); PIN(a1); w1[s & 1] = s < 31 ? WF(0, s + 2, 1) : WF(1, (32 - s) ^ 1, 1); SBAR();
      a2 = MFMA32(w2[s & 1], hf[s], a2); PIN(a2); if (s < 31) w2[s & 1] = WF(0, s + 2, 2);
      if (s == 30) { const u32x4 sv = *(LAS u32x4*)(lds + P4_ST + cw * 512 + t * 16);
        ca = __uint_as_float(sv[0]); cbb = __uint_as_float(sv[1]); ve = __uint_as_float(sv[2]);
        hf[32] = as_frag((u32x4){sv[3], sv[3], sv[3], sv[3]}); }
      SBAR();
    });
    if (w == 4) STAMP(13);
    float g0, g1, u0, u1;
#define PIN2(a, b) asm volatile("" : "+v"(a), "+v"(b))
#define EPI5(acc, q, k) do { \
      if ((k) == 0) { u0 = __builtin_fmaf(acc[2 * (q)] * acc[2 * (q)], cbb, ca); u1 = __builtin_fmaf(acc[2 * (q) + 1] * acc[2 * (q) + 1], cbb, ca); PIN2(u0, u1); } \
      else if ((k) == 1) { g0 = __builtin_amdgcn_exp2f(acc[2 * (q)] * u0); g1 = acc[2 * (q) + 1] * u1; PIN2(g0, g1); } \
      else if ((k) == 2) { g1 = __builtin_amdgcn_exp2f(g1); g0 = __builtin_fmaf(g0, ve, ve); u0 = acc[2 * (q)] * acc[8 + 2 * (q)]; asm volatile("" : "+v"(g0), "+v"(g1), "+v"(u0)); } \
      else if ((k) == 3) { g0 = __builtin_amdgcn_rcpf(g0); g1 = __builtin_fmaf(g1, ve, ve); u1 = acc[2 * (q) + 1] * acc[9 + 2 * (q)]; asm volatile("" : "+v"(g0), "+v"(g1), "+v"(u1)); } \
      else { g1 = __builtin_amdgcn_rcpf(g1); u0 *= g0; PIN2(g1, u0); ov[q] = pk2(u0, u1 * g1); asm volatile("" : "+v"(ov[q])); } } while (0)
#define EPI_B(gap) do { constexpr int e_ = (gap) >= 3 ? ((gap) - 3) / 5 : 99, k_ = (gap) >= 3 ? ((gap) - 3) % 5 : 0; if constexpr (e_ < 12) { \
      if (e_ < 4) EPI5(a0, e_ & 3, k_); else if (e_ < 8) EPI5(a1, e_ & 3, k_); else EPI5(a2, e_ & 3, k_); \
      if (k_ == 4 && (e_ & 3) == 3) stc16(rws, l16, ab + (e_ >> 2) * 1024, ov); } } while (0)
#define EPI_C(gap) do { constexpr int e_ = (gap) >= 3 ? ((gap) - 3) / 5 : 99, k_ = (gap) >= 3 ? ((gap) - 3) % 5 : 0; if constexpr (e_ < 8) { \
      if (e_ < 4) EPI5(b0, e_ & 3, k_); else EPI5(b1, e_ & 3, k_); \
      if (k_ == 4 && (e_ & 3) == 3) stc16(rws, l16, ab3 + (e_ >> 2) * 1024, ov); } } while (0)
    char* f4 = sync + SY_F4 + (mt * 16 + cb4) * 128;
    sfor<0, 33>([&](auto S) __attribute__((always_inline)) { constexpr int s = decltype(S)::v;
      if (s == 11 || s == 31) BARRIER();
      SBAR();
      b0 = MFMA32(w0[(s + 1) & 1], hf[s], b0); PIN(b0); w0[(s + 1) & 1] = s < 31 ? WF(1, s + 2, 0) : WF(2, s - 31, 0); EPI_B(2 * s); SBAR();
      b1 = MFMA32(w1[(s + 1) & 1], hf[s], b1); PIN(b1); w1[(s + 1) & 1] = s < 31 ? WF(1, s + 2, 1) : WF(2, s - 31, 1); EPI_B(2 * s + 1); SBAR();
    });
    if (w == 4) STAMP(14);
    a0 = (f32x16){}; a1 = (f32x16){};
    sfor<0, 33>([&](auto S) __attribute__((always_inline)) { constexpr int s = decltype(S)::v;
      if (s == 10) { WAITV(0); flag_set(f4, tag, (int)l16); SBAR(); }
      a0 = MFMA32(w0[s & 1], hf[s], a0); PIN(a0); if (s < 31) w0[s & 1] = WF(2, s + 2, 0); EPI_C(2 * s); SBAR();
      a1 = MFMA32(w1[s & 1], hf[s], a1); PIN(a1); if (s < 31) w1[s & 1] = WF(2, s + 2, 1); EPI_C(2 * s + 1); SBAR();
      if (s == 30) lds_signal(ctl + C_P4, (int)l16);
    });
    if (w == 4) STAMP(15);
    WAITV(0); flag_set(f4 + 64 * 2048, tag, (int)l16);
#pragma unroll
    for (int q = 0; q < 4; ++q) EPI(a0, q);
    stc16(rws, l16, ab5, ov);
#pragma unroll
    for (int q = 0; q < 4; ++q) EPI(a1, q);
    stc16(rws, l16, ab5 + 1024, ov);
    if (w == 4) STAMP(16);
    WAITV(0); flag_set(f4 + 128 * 2048, tag, (int)l16);
    if (w == 4) STAMP(17);
    { RELANE();
    if (cw >= 2) {
      char* f5 = sync + SY_F4 + (64 + rb3 * 2 + ((lane >> 3) & 1)) * 2048 + (cw - 2) * 1024 + (lane & 7) * 128;
      poll1(f5, tag); lds_signal(ctl + C_GB + (cw - 2) * 4, (int)l16);
      __builtin_amdgcn_s_sleep(DLY / 2);
      poll1(f5 + 64 * 2048, tag); lds_signal(ctl + C_GB + (cw - 2) * 4, (int)l16);
      if (w == 6) STAMP(25);
    } else {
      const char* asrc = ws + WS_A + ((size_t)(rb3 * 2 * 112 + cw * 56) << 10);
#define P5_DMA(r) do { dma4c(l16, asrc + (r) * 4096, (unsigned)(p5_slot(r) + cw * 8192)); dma4c(l16, asrc + 112 * 1024 + (r) * 4096, (unsigned)(p5_slot(r) + cw * 8192 + 4096)); } while (0)
      auto pair_wait = [&](int off, unsigned n) __attribute__((always_inline)) {
_Pragma("unroll 1")
        for (unsigned spin = 0; spin < SPIN_LDS; ++spin) { const u32x2 v = *(volatile LAS u32x2*)(ctl + off + cw * 8); if (__builtin_amdgcn_readfirstlane(min(v[0], v[1])) >= n) break; __builtin_amdgcn_s_sleep(1); }
        asm volatile("" ::: "memory"); };
#define done_wait(n) pair_wait(C_DONE, n)
      unsigned a_rdy = lane < 2 ? (unsigned)(LDS_CTL + C_RDY + cw * 8 + lane * 4) : (unsigned)(LDS_CTL + 768 + lane * 4); asm volatile("" : "+v"(a_rdy));
#define P5_RDY(n) word_set(lds, a_rdy, n)
      pair_wait(C_RDY, 3); P5_DMA(5);
      lds_wait_slow(ctl + C_GB + cw * 4, 1);
      if (w == 4) STAMP(27);
      pair_wait(C_RDY, 4); P5_DMA(6);
      pair_wait(C_RDY, 5); P5_DMA(7);
      WAITV(16); P5_RDY(6); done_wait(4); P5_DMA(8);
      WAITV(16); P5_RDY(7); done_wait(5); P5_DMA(9);
      if (w == 4) STAMP(24);
      WAITV(16); P5_RDY(8);
      lds_wait_slow(ctl + C_GB + cw * 4, 2);
      P5_DMA(10);
      WAITV(16); P5_RDY(9); done_wait(6); P5_DMA(11);
      WAITV(16); P5_RDY(10); P5_DMA(12);
      WAITV(16); P5_RDY(11); done_wait(7); P5_DMA(13);
      if (w == 4) STAMP(28);
      WAITV(16); P5_RDY(12); WAITV(8); P5_RDY(13); WAITV(0); P5_RDY(14);
    }
    }
    STAMP_FLUSH();
    if (bid == 0 && w == 4 && l16 == 0) __hip_atomic_store((GAS u64*)(sync + SY_EPOCH), tag, RLX);
    asm volatile("s_endpgm" ::: "memory");
#pragma unroll
    for (int j = 0; j < NWD; ++j) asm volatile("" : "=v"(wdr[j]));
  }
  {
    int lw = w; asm volatile("" : "+s"(lw));
    RELANE();
    const int n5 = lw & 1, k5 = lw >> 1;
    lds_wait_slow(ctl + C_H + lw * 4, 1);
    u32x2 st[4];
#pragma unroll
    for (int j = 0; j < 4; ++j) st[j] = ldc8(rws, lane * 8 + j * 512, WS_HST + (rb4 * 4 + lw) * 2048);
    lds_wait(ctl + C_F3, 4); lds_wait(ctl + C_F3 + 4, 4);
#define GU_G(e) dma4c(l16, gsrc + (93 + (e) * 16 + lw * 4) * 1024, (unsigned)((93 + (e) * 16 + lw * 4) * 1024))
    GU_G(0);
    {
      float s1 = 0.f, s2 = 0.f;
#pragma unroll
      for (int j = 0; j < 4; ++j) { s1 += __uint_as_float(st[j][0]); s2 += __uint_as_float(st[j][1]); }
      s1 = swap32_add(s1); s2 = swap32_add(s2);
      const float mean = s1 * (1.f / DM), ve = fmaxf(s2 * (1.f / DM) - mean * mean, 0.f) + LN_EPS, rstd = __builtin_amdgcn_rsqf(ve);
      if (lane < 32) *(LAS u32x4*)(lds + P4_ST + lw * 512 + t * 16) = (u32x4){__float_as_uint(-2.3022081986f * rstd), __float_as_uint(-0.1029432397f * rstd * rstd * rstd), __float_as_uint(ve),
                                                                             pk2(-mean, -mean - bfhi(pk2(-mean, -mean)))};
    }
    if (w == 0) STAMP(30);
    GU_G(1);
    WAITV(4); BARRIER();
    GU_G(2); WAITV(4); GU_G(3);
    dma2c(l16, gsrc + (157 + lw * 2) * 1024, (unsigned)(lw * 2048));
    BARRIER();
    if (w == 0) STAMP(31);
#define GU_C(e) dma4c(l16, gsrc + (165 + (e) * 16 + lw * 4) * 1024, (unsigned)((8 + (e) * 16 + lw * 4) * 1024))
    WAITV(2); GU_C(0); WAITV(4); GU_C(1);
    BARRIER();
    WAITV(PACE); GU_C(2); WAITV(PACE); GU_C(3);
    dma16c(l16, gsrc + (229 + (lw & 1)) * 1024, (unsigned)((72 + (lw & 1)) * 1024));
    WAITV(PACE); WDR(NWD, NWD + 4); WAITV(4); WDR(NWD + 4, NWD + 8);
    BARRIER();
    if (w == 0) STAMP(26);
#pragma unroll
    for (int r = 0; r < 4; ++r) { dma4c(l16, ws + wd_src + ((16 + r * 4) << 10), (unsigned)(P5_W + r * 16384 + lw * 4096)); WAITV(PACE); }
    poll(sync + SY_F4 + (rb3 * 2 + n5) * 2048 + k5 * 1024, 8, tag, lane);
    if (w == 0) STAMP(18);
    const char* asrc = ws + WS_A + ((size_t)((rb3 * 2 + n5) * 112 + k5 * 56) << 10);
    if (l16 == 0) __hip_atomic_store((LAS unsigned*)(ctl + C_GO + lw * 4), 1u, __ATOMIC_RELAXED, __HIP_MEMORY_SCOPE_WORKGROUP);
#define P5_SELF(r) dma4c(l16, asrc + (r) * 4096, (unsigned)(p5_slot(r) + lw * 4096))
    P5_SELF(0);
    lds_wait_slow(ctl + C_P4, 4);
    P5_SELF(1); P5_SELF(2);
    if (w == 0) STAMP(29);
    f32x16 c0 = {}, c1 = {};
    unsigned ap = l16 + k5 * 8192, ap0 = ap + 78 * 1024; asm volatile("" : "+v"(ap), "+v"(ap0));
    unsigned wp = P5_W + lw * 4096 + l16, ap1 = ap + P5_W; asm volatile("" : "+v"(wp), "+v"(ap1));
#define P5_A(ks, m) lds_frag(lds + (p5_slot((ks) >> 2) < 65536 ? ap + p5_slot((ks) >> 2) : p5_slot((ks) >> 2) < P5_W ? ap0 : ap1 + (p5_slot((ks) >> 2) - P5_W)) + (m) * 4096 + ((ks) & 3) * 1024)
    unsigned seen = 0; u64 peek = 0;
#define RDY_LOAD() __hip_atomic_load((LAS u64*)(ctl + C_RDY + k5 * 8), __ATOMIC_RELAXED, __HIP_MEMORY_SCOPE_WORKGROUP)
#define RDY_MIN(v) __builtin_amdgcn_readfirstlane(min((unsigned)(v), (unsigned)((v) >> 32)))
    auto rdy_wait = [&](unsigned n) __attribute__((always_inline)) {
      if (__builtin_expect(seen < n, 0)) {
_Pragma("unroll 1")
        for (unsigned spin = 0; spin < SPIN_LDS; ++spin) { const u64 v = RDY_LOAD(); seen = RDY_MIN(v); if (seen >= n) break; } }
      asm volatile("" ::: "memory"); };
    const unsigned a_rdy = word_addr(C_RDY + lw * 4, lane), a_done = word_addr(C_DONE + lw * 4, lane);
    WAITV(8); P5_SELF(3); word_set(lds, a_rdy, 1); rdy_wait(1);
    bf16x8 f0[2] = {P5_A(0, 0), P5_A(1, 0)}, f1[2] = {P5_A(0, 1), P5_A(1, 1)}, fw[2];
    sfor<0, 56>([&](auto K) __attribute__((always_inline)) { constexpr int ks = decltype(K)::v;
      if constexpr ((ks & 3) == 1 && ks < 16) {
        WAITV(ks < 8 ? 8 : ks == 9 ? 4 : 0); if constexpr (ks == 1) P5_SELF(4); word_set(lds, a_rdy, (ks >> 2) + 2); }
      if constexpr ((ks & 3) == 1 && ks + 3 < 56) peek = RDY_LOAD();
      if constexpr ((ks & 3) == 2 && ks < 28) word_set(lds, a_done, (ks >> 2) + 1);
      if constexpr ((ks & 3) == 2 && ks + 2 < 56) { seen = RDY_MIN(peek); rdy_wait((ks >> 2) + 2); if (w == 0 && (ks == 2 || ks == 22 || ks == 38)) STAMP(32 + (ks >> 2)); }
      SBAR();
      const bf16x8 f = ks < 16 ? wdr[ks < 16 ? ks : 0] : ks < 32 ? fw[ks & 1] : wdr[ks < 32 ? 0 : ks - 16];
      c0 = MFMA32(f, f0[ks & 1], c0); PIN(c0); if constexpr (ks + 2 < 56) f0[ks & 1] = P5_A(ks + 2, 0); SBAR();
      c1 = MFMA32(f, f1[ks & 1], c1); PIN(c1);
      if constexpr (ks == 20) dma2c(l16, ws + WS_H + (((rb3 * 2 + k5) * 32 + (cb3 * 2 + n5) * 2) << 10), (unsigned)(65536 + lw * 2048));
      if constexpr (ks >= 16 && (ks & 3) == 0 && NWD + 8 + (ks - 16) < 40) WDR(NWD + 8 + (ks - 16), NWD + 12 + (ks - 16));
      if constexpr (ks + 2 < 56) { f1[ks & 1] = P5_A(ks + 2, 1); if constexpr (ks + 2 >= 16 && ks + 2 < 32) fw[ks & 1] = lds_frag(lds + wp + ((ks + 2 - 16) >> 2) * 16384 + ((ks + 2) & 3) * 1024); }
      SBAR();
    });
    if (w == 0) STAMP(19);
    WAITV(0);
    { RELANE();
    { const f32x16& s = k5 ? c0 : c1;
#pragma unroll
      for (int i = 0; i < 4; ++i) *(LAS f32x4*)(lds + wp + 49152 + i * 1024) = (f32x4){s[4 * i], s[4 * i + 1], s[4 * i + 2], s[4 * i + 3]}; }
    lds_signal(ctl + C_X, (int)l16); lds_wait(ctl + C_X, 4);
    { const f32x16& s = k5 ? c1 : c0; lptr xp = lds + P5_W + 49152 + (lw ^ 2) * 4096 + l16;
#pragma unroll
      for (int i = 0; i < 2; ++i) { const f32x4 a = *(LAS f32x4*)(xp + i * 2048), b = *(LAS f32x4*)(xp + i * 2048 + 1024);
        const u32x4 rh = *(LAS u32x4*)(lds + 65536 + lw * 2048 + i * 1024 + l16);
        const unsigned d0 = pk2(s[8 * i] + a[0] + bflo(rh[0]), s[8 * i + 1] + a[1] + bfhi(rh[0])), d1 = pk2(s[8 * i + 2] + a[2] + bflo(rh[1]), s[8 * i + 3] + a[3] + bfhi(rh[1]));
        const unsigned d2 = pk2(s[8 * i + 4] + b[0] + bflo(rh[2]), s[8 * i + 5] + b[1] + bfhi(rh[2])), d3 = pk2(s[8 * i + 6] + b[2] + bflo(rh[3]), s[8 * i + 7] + b[3] + bfhi(rh[3]));
        auto r0 = __builtin_amdgcn_permlane32_swap(d0, d2, false, false); auto r1 = __builtin_amdgcn_permlane32_swap(d1, d3, false, false);
        *(u32x4*)(out + (size_t)((rb3 * 2 + k5) * 32 + t) * DM + (cb3 * 2 + n5) * 32 + i * 16 + h * 8) = (u32x4){r0[0], r1[0], r0[1], r1[1]}; } }
    }
    if (w == 0) STAMP(20);
    STAMP_FLUSH();
  }
}

extern "C" void kernel_launch(void* const* d_in, const int* in_sizes, int n_in, void* d_out, int out_size, void* d_ws, size_t ws_size, hipStream_t stream) {
  (void)hipFuncSetAttribute((const void*)k_block, hipFuncAttributeMaxDynamicSharedMemorySize, LDS_BYTES);
  hipLaunchKernelGGL(k_block, dim3(256), dim3(512), LDS_BYTES, stream, (const uint16_t*)d_in[0], (const uint16_t*)d_in[1], (const uint16_t*)d_in[2],
                     (const uint16_t*)d_in[4], (const uint16_t*)d_in[5], (const uint16_t*)d_in[6], (char*)d_ws, (uint16_t*)d_out);
}
```
